```python
import torch

def _softmax(x, axis=-1):
    return torch.softmax(x, dim=axis)
def _gelu(x):
    return torch.nn.functional.gelu(x, approximate="tanh")
def _silu(x):
    return torch.nn.functional.silu(x)
def _sigmoid(x):
    return torch.sigmoid(x)
def _layernorm(x, axis=-1, eps=1e-5):
    m = x.mean(dim=axis, keepdim=True)
    v = x.var(dim=axis, keepdim=True, unbiased=False)
    return (x - m) / torch.sqrt(v + eps)
def _scatter_add(tgt, idx, src, axis=0):
    return tgt.clone().index_add_(axis, idx, src)
def _segment_sum(src, seg, n):
    out = torch.zeros((n,) + src.shape[1:], dtype=src.dtype)
    return out.index_add_(0, seg, src)
def _pad(x, pw):
    flat = tuple(v for pair in reversed(pw) for v in pair)
    return torch.nn.functional.pad(x, flat)

kernel_name = 'standalone__dense_activation_silu__prompt'

def setup_inputs():

    _w = (lambda a: (lambda t: t.to(torch.bfloat16) if t.is_floating_point() else t)(torch.as_tensor(a)))
    B, S, D, F = 1, 2048, 640, 1728
    return {
        "x": _w((lambda *s: torch.randn(*s, dtype=torch.bfloat16) * 0.1)(B, S, D)),
        "w1": _w((lambda *s: torch.randn(*s, dtype=torch.bfloat16) * 3.2)(D, F) * (D ** -0.5)),
        "w2": _w((lambda *s: torch.randn(*s, dtype=torch.bfloat16) * 3.2)(F, D) * (F ** -0.5)),
    }

def reference(x, w1, w2):
    h = torch.einsum("bsd,df->bsf", x, w1)
    h = _silu(h)
    out = torch.einsum("bsf,fd->bsd", h, w2)
    return out
```

```cpp
#include <hip/hip_runtime.h>
#include <hip/hip_bf16.h>
#include <stdint.h>
#include <type_traits>

typedef unsigned short u16;
typedef __attribute__((ext_vector_type(8))) short bf16x8;
typedef __attribute__((ext_vector_type(4))) short s16x4;
typedef __attribute__((ext_vector_type(4))) float f32x4;
typedef __attribute__((ext_vector_type(2))) unsigned u32x2;
typedef __attribute__((address_space(3))) const char* lds_cptr;

constexpr int M_ = 2048, D_ = 640, F_ = 1728;

template <int I, int N, class Fn>
__device__ __forceinline__ void static_for(Fn&& f) {
    if constexpr (I < N) { f(std::integral_constant<int, I>{}); static_for<I + 1, N>(f); }
}
template <int N> __device__ __forceinline__ void wait_vm() { asm volatile("s_waitcnt vmcnt(%0)" ::"n"(N) : "memory"); }
__device__ __forceinline__ void wait_lgkm0() { asm volatile("s_waitcnt lgkmcnt(0)" ::: "memory"); }

template <int IMM>
__device__ __forceinline__ void glds16(const void* sbase, unsigned voff, unsigned lds_base) {
    unsigned keep;
    asm volatile("s_mov_b32 %0, m0\n\ts_mov_b32 m0, %3\n\ts_nop 0\n\tglobal_load_lds_dwordx4 %1, %2 offset:%4\n\ts_mov_b32 m0, %0"
                 : "=&s"(keep) : "v"(voff), "s"(sbase), "s"(lds_base), "n"(IMM) : "memory");
}
__device__ __forceinline__ bf16x8 lds_b128(lds_cptr p) { return *(const __attribute__((address_space(3))) bf16x8*)p; }
__device__ __forceinline__ s16x4 lds_tr(lds_cptr p) {
    return __builtin_amdgcn_ds_read_tr16_b64_v4i16((__attribute__((address_space(3))) s16x4*)p);
}
__device__ __forceinline__ bf16x8 cat8(s16x4 lo, s16x4 hi) { return __builtin_shufflevector(lo, hi, 0, 1, 2, 3, 4, 5, 6, 7); }
#define MFMA16(a, b, c) __builtin_amdgcn_mfma_f32_16x16x32_bf16(a, b, c, 0, 0, 0)

__device__ __forceinline__ float bf16_round(float v) { return (float)(__bf16)v; }
__device__ __forceinline__ unsigned pack2(float lo, float hi) {
    typedef __attribute__((ext_vector_type(2))) float f32x2;
    typedef __attribute__((ext_vector_type(2))) __bf16 bf16x2;
    f32x2 v = {lo, hi};
    bf16x2 r = __builtin_convertvector(v, bf16x2);
    return __builtin_bit_cast(unsigned, r);
}
__device__ __forceinline__ float silu_f(float v) { return v * __builtin_amdgcn_rcpf(1.0f + __expf(-v)); }

constexpr int G1_NS = 5, G1_NK = D_ / 64;
constexpr int G1_A = 128 * 128, G1_B = 64 * 224, G1_ST = G1_A + G1_B;

__global__ __launch_bounds__(256, 1) void gemm1_kernel(const u16* __restrict__ x, const u16* __restrict__ w1, u16* __restrict__ h) {
    __shared__ __attribute__((aligned(16))) char smem[G1_ST * G1_NS];
    const int tid = threadIdx.x, lane = tid & 63;
    const int wid = __builtin_amdgcn_readfirstlane(tid >> 6);
    const int b = blockIdx.x, xcd = b & 7, idx = b >> 3;
    const int mb = (xcd >> 1) * 4 + (idx & 3), nb = (xcd & 1) * 8 + (idx >> 2);
    const int m0 = mb * 128, n0 = nb * 112;
    const unsigned lds0 = (unsigned)(uintptr_t)smem;

    unsigned voff[8];
    const char* sbase;
    unsigned ldst;
    unsigned kadv;
    if (wid < 2) {
#pragma unroll
        for (int i = 0; i < 8; ++i) {
            int row = wid * 64 + i * 8 + (lane >> 3);
            int ch = (lane & 7) ^ ((row >> 1) & 7);
            voff[i] = (unsigned)(row * (D_ * 2) + ch * 16);
        }
        sbase = (const char*)(x + (size_t)m0 * D_);
        ldst = wid * 8192;
        kadv = 128;
    } else {
#pragma unroll
        for (int i = 0; i < 7; ++i) {
            int c = ((wid - 2) * 7 + i) * 64 + lane;
            int rho = c / 14, nc = c - rho * 14;
            int k = rho ^ (((rho >> 3) & 1) << 2);
            int col = n0 + nc * 8; col = col > F_ - 8 ? F_ - 8 : col;
            voff[i] = (unsigned)(k * (F_ * 2) + col * 2);
        }
        voff[7] = 0;
        sbase = (const char*)w1;
        ldst = G1_A + (wid - 2) * 7168;
        kadv = 64 * F_ * 2;
    }
    auto issue = [&](int stage) {
        const char* sb = sbase + (size_t)stage * kadv;
        unsigned dst = lds0 + (stage % G1_NS) * G1_ST + ldst;
#pragma unroll
        for (int i = 0; i < 7; ++i) glds16<0>(sb, voff[i], dst + i * 1024);
        if (wid < 2) glds16<0>(sb, voff[7], dst + 7 * 1024);
    };

    const int r = lane & 15, g = lane >> 4, q = (lane & 15) >> 2, p = lane & 3;
    lds_cptr xa[2];
#pragma unroll
    for (int mt = 0; mt < 2; ++mt) {
        int row = wid * 32 + mt * 16 + r;
        xa[mt] = (lds_cptr)smem + row * 128 + ((g ^ ((row >> 1) & 7)) * 16);
    }
    lds_cptr wa[2];
#pragma unroll
    for (int t = 0; t < 2; ++t) wa[t] = (lds_cptr)smem + G1_A + (8 * g + 4 * (t ^ (g & 1)) + q) * 224 + p * 8;

    f32x4 acc[2][7];
#pragma unroll
    for (int mt = 0; mt < 2; ++mt)
#pragma unroll
        for (int ft = 0; ft < 7; ++ft) acc[mt][ft] = (f32x4){0.f, 0.f, 0.f, 0.f};

    bf16x8 xf[2][2], wf[2][7];
    auto ldfrag = [&](int buf, int stage, int s) {
        const int so = (stage % G1_NS) * G1_ST;
#pragma unroll
        for (int mt = 0; mt < 2; ++mt) {
            lds_cptr a = (lds_cptr)((unsigned)(uintptr_t)xa[mt] ^ (unsigned)(s * 64));
            xf[buf][mt] = lds_b128(a + so);
        }
#pragma unroll
        for (int ft = 0; ft < 7; ++ft)
            wf[buf][ft] = cat8(lds_tr(wa[0] + so + s * 32 * 224 + ft * 32), lds_tr(wa[1] + so + s * 32 * 224 + ft * 32));
    };
    auto mma = [&](int buf) {
#pragma unroll
        for (int ft = 0; ft < 7; ++ft)
#pragma unroll
            for (int mt = 0; mt < 2; ++mt) acc[mt][ft] = MFMA16(wf[buf][ft], xf[buf][mt], acc[mt][ft]);
    };

    issue(0); issue(1); issue(2); issue(3);
    if (wid < 2) wait_vm<24>(); else wait_vm<21>();
    __builtin_amdgcn_s_barrier();
    ldfrag(0, 0, 0);
    static_for<0, G1_NK>([&](auto KT) {
        constexpr int kt = KT.value;
        ldfrag(1, kt, 1);
        mma(0);
        if constexpr (kt + 1 < G1_NK) {
            constexpr int last_issued = (kt + 3 < G1_NK - 1) ? kt + 3 : G1_NK - 1;
            constexpr int nout = last_issued - (kt + 1);
            if (wid < 2) wait_vm<8 * nout>(); else wait_vm<7 * nout>();
            __builtin_amdgcn_s_barrier();
            if constexpr (kt + 4 < G1_NK) issue(kt + 4);
            ldfrag(0, kt + 1, 0);
        }
        mma(1);
    });

#pragma unroll
    for (int mt = 0; mt < 2; ++mt) {
        u16* hrow = h + (size_t)(m0 + wid * 32 + mt * 16 + r) * F_ + n0 + 4 * g;
#pragma unroll
        for (int ft = 0; ft < 7; ++ft) {
            if (n0 + ft * 16 < F_) {
                f32x4 v = acc[mt][ft];
                u32x2 o;
                o.x = pack2(silu_f(bf16_round(v[0])), silu_f(bf16_round(v[1])));
                o.y = pack2(silu_f(bf16_round(v[2])), silu_f(bf16_round(v[3])));
                *(u32x2*)(hrow + ft * 16) = o;
            }
        }
    }
}

constexpr int G2_A = 64 * 64, G2_B = 32 * 160, G2_ST = G2_A + G2_B, G2_R = 4, G2_W = G2_ST * G2_R;

template <int NST>
__device__ __forceinline__ void gemm2_wave(const u16* __restrict__ h, const u16* __restrict__ w2, char* smem, int wid, int lane,
                                           int m0, int n0, int ks0, f32x4 (&acc)[4][5]) {
    const unsigned ldsw = (unsigned)(uintptr_t)smem + wid * G2_W;
    unsigned va[4], vb[5];
#pragma unroll
    for (int i = 0; i < 4; ++i) {
        int row = i * 16 + (lane >> 2);
        int ch = (lane & 3) ^ ((-(row >> 2)) & 3);
        va[i] = (unsigned)(row * (F_ * 2) + ch * 16);
    }
#pragma unroll
    for (int i = 0; i < 5; ++i) {
        int c = i * 64 + lane;
        int rho = c / 10, nc = c - rho * 10;
        int k = rho ^ (((rho >> 3) & 1) << 2);
        vb[i] = (unsigned)(k * (D_ * 2) + nc * 16);
    }
    const char* sa = (const char*)(h + (size_t)m0 * F_ + ks0 * 32);
    const char* sb = (const char*)(w2 + (size_t)ks0 * 32 * D_ + n0);
    auto issue = [&](int s) {
        const char* a = sa + s * 64;
        const char* bb = sb + (size_t)s * (32 * D_ * 2);
        unsigned dst = ldsw + (s % G2_R) * G2_ST;
#pragma unroll
        for (int i = 0; i < 4; ++i) glds16<0>(a, va[i], dst + i * 1024);
#pragma unroll
        for (int i = 0; i < 5; ++i) glds16<0>(bb, vb[i], dst + G2_A + i * 1024);
    };
    const int r = lane & 15, g = lane >> 4, q = (lane & 15) >> 2, p = lane & 3;
    lds_cptr base = (lds_cptr)smem + wid * G2_W;
    lds_cptr ha = base + r * 64 + ((g ^ ((-(r >> 2)) & 3)) * 16);
    lds_cptr wa[2];
#pragma unroll
    for (int t = 0; t < 2; ++t) wa[t] = base + G2_A + (8 * g + 4 * (t ^ (g & 1)) + q) * 160 + p * 8;

    bf16x8 hf[2][4], wf[2][5];
    auto ldfrag = [&](int buf, int s) {
        const int so = (s % G2_R) * G2_ST;
#pragma unroll
        for (int mt = 0; mt < 4; ++mt) hf[buf][mt] = lds_b128(ha + so + mt * 1024);
#pragma unroll
        for (int ft = 0; ft < 5; ++ft) wf[buf][ft] = cat8(lds_tr(wa[0] + so + ft * 32), lds_tr(wa[1] + so + ft * 32));
    };
    auto mma = [&](int buf) {
#pragma unroll
        for (int ft = 0; ft < 5; ++ft)
#pragma unroll
            for (int mt = 0; mt < 4; ++mt) acc[mt][ft] = MFMA16(wf[buf][ft], hf[buf][mt], acc[mt][ft]);
    };
    issue(0); issue(1); issue(2); issue(3);
    wait_vm<27>();
    ldfrag(0, 0);
    static_for<0, NST>([&](auto S) {
        constexpr int s = S.value;
        if constexpr (s + 1 < NST) {
            constexpr int last_issued = (s + 3 < NST - 1) ? s + 3 : NST - 1;
            wait_vm<9 * (last_issued - (s + 1))>();
            wait_lgkm0();
            if constexpr (s + 4 < NST) issue(s + 4);
            ldfrag((s + 1) & 1, s + 1);
        }
        mma(s & 1);
    });
}

__global__ __launch_bounds__(256, 1) void gemm2_kernel(const u16* __restrict__ h, const u16* __restrict__ w2, u16* __restrict__ out) {
    __shared__ __attribute__((aligned(16))) char smem[G2_W * 4];
    const int tid = threadIdx.x, lane = tid & 63;
    const int wid = __builtin_amdgcn_readfirstlane(tid >> 6);
    const int b = blockIdx.x, xcd = b & 7, idx = b >> 3;
    const int mb = (xcd >> 1) * 8 + (idx & 7), nb = (xcd & 1) * 4 + (idx >> 3);
    const int m0 = mb * 64, n0 = nb * 80;
    f32x4 acc[4][5];
#pragma unroll
    for (int mt = 0; mt < 4; ++mt)
#pragma unroll
        for (int ft = 0; ft < 5; ++ft) acc[mt][ft] = (f32x4){0.f, 0.f, 0.f, 0.f};
    if (wid < 2) gemm2_wave<14>(h, w2, smem, wid, lane, m0, n0, wid * 14, acc);
    else gemm2_wave<13>(h, w2, smem, wid, lane, m0, n0, 28 + (wid - 2) * 13, acc);

    wait_lgkm0();
    f32x4* mine = (f32x4*)(smem + wid * G2_W);
#pragma unroll
    for (int mt = 0; mt < 4; ++mt)
        if (mt != wid) {
#pragma unroll
            for (int ft = 0; ft < 5; ++ft) mine[(mt * 5 + ft) * 64 + lane] = acc[mt][ft];
        }
    __syncthreads();
    f32x4 res[5];
#pragma unroll
    for (int ft = 0; ft < 5; ++ft) res[ft] = (wid == 0) ? acc[0][ft] : (wid == 1) ? acc[1][ft] : (wid == 2) ? acc[2][ft] : acc[3][ft];
#pragma unroll
    for (int w = 0; w < 4; ++w)
        if (w != wid) {
            const f32x4* o = (const f32x4*)(smem + w * G2_W);
#pragma unroll
            for (int ft = 0; ft < 5; ++ft) res[ft] += o[(wid * 5 + ft) * 64 + lane];
        }
    const int r = lane & 15, g = lane >> 4;
    u16* orow = out + (size_t)(m0 + wid * 16 + r) * D_ + n0 + 4 * g;
#pragma unroll
    for (int ft = 0; ft < 5; ++ft) {
        u32x2 o;
        o.x = pack2(res[ft][0], res[ft][1]);
        o.y = pack2(res[ft][2], res[ft][3]);
        *(u32x2*)(orow + ft * 16) = o;
    }
}

extern "C" void kernel_launch(void* const* d_in, const int* in_sizes, int n_in, void* d_out, int out_size, void* d_ws,
                              size_t ws_size, hipStream_t stream) {
    const u16* x = (const u16*)d_in[0];
    const u16* w1 = (const u16*)d_in[1];
    const u16* w2 = (const u16*)d_in[2];
    u16* h = (u16*)d_ws;
    u16* out = (u16*)d_out;
    gemm1_kernel<<<256, 256, 0, stream>>>(x, w1, h);
    gemm2_kernel<<<256, 256, 0, stream>>>(h, w2, out);
}
```

```cpp
#include <hip/hip_runtime.h>
#include <hip/hip_bf16.h>
#include <stdint.h>
#include <type_traits>

typedef unsigned short u16;
typedef __attribute__((ext_vector_type(8))) short bf16x8;
typedef __attribute__((ext_vector_type(4))) short s16x4;
typedef __attribute__((ext_vector_type(4))) float f32x4;
typedef __attribute__((ext_vector_type(2))) unsigned u32x2;
typedef __attribute__((address_space(3))) const char* lds_cptr;

constexpr int M_ = 2048, D_ = 640, F_ = 1728;

template <int I, int N, class Fn>
__device__ __forceinline__ void static_for(Fn&& f) {
    if constexpr (I < N) { f(std::integral_constant<int, I>{}); static_for<I + 1, N>(f); }
}
template <int N> __device__ __forceinline__ void wait_vm() { asm volatile("s_waitcnt vmcnt(%0)" ::"n"(N) : "memory"); }

__device__ __forceinline__ void glds16(const void* sbase, unsigned voff, unsigned lds_base) {
    unsigned keep;
    asm volatile("s_mov_b32 %0, m0\n\ts_mov_b32 m0, %3\n\ts_nop 0\n\tglobal_load_lds_dwordx4 %1, %2\n\ts_mov_b32 m0, %0"
                 : "=&s"(keep) : "v"(voff), "s"(sbase), "s"(lds_base) : "memory");
}
__device__ __forceinline__ bf16x8 lds_b128(lds_cptr p) { return *(const __attribute__((address_space(3))) bf16x8*)p; }
__device__ __forceinline__ s16x4 lds_tr(lds_cptr p) {
    return __builtin_amdgcn_ds_read_tr16_b64_v4i16((__attribute__((address_space(3))) s16x4*)p);
}
__device__ __forceinline__ bf16x8 cat8(s16x4 lo, s16x4 hi) { return __builtin_shufflevector(lo, hi, 0, 1, 2, 3, 4, 5, 6, 7); }
#define MFMA16(a, b, c) __builtin_amdgcn_mfma_f32_16x16x32_bf16(a, b, c, 0, 0, 0)

__device__ __forceinline__ float bf16_round(float v) { return (float)(__bf16)v; }
__device__ __forceinline__ unsigned pack2(float lo, float hi) {
    typedef __attribute__((ext_vector_type(2))) float f32x2;
    typedef __attribute__((ext_vector_type(2))) __bf16 bf16x2;
    f32x2 v = {lo, hi};
    bf16x2 r = __builtin_convertvector(v, bf16x2);
    return __builtin_bit_cast(unsigned, r);
}
__device__ __forceinline__ float silu_f(float v) { return v * __builtin_amdgcn_rcpf(1.0f + __expf(-v)); }

template <int BM, int BN, int K, int N, int NS, bool SILU>
__device__ __forceinline__ void gemm_tile(const u16* __restrict__ A, const u16* __restrict__ B, u16* __restrict__ C,
                                          char* smem, int m0, int n0) {
    constexpr int NK = K / 64;
    constexpr int AB = BM * 128, BB = 64 * BN * 2, ST = AB + BB;
    constexpr int NA = BM / 8, NB = BB / 1024, NI = (NA + NB) / 4;
    static_assert((NA + NB) % 4 == 0, "pieces");
    constexpr int WM = BM / 2, WN = BN / 2, TM = WM / 16, TN = WN / 16;
    const int tid = threadIdx.x, lane = tid & 63;
    const int wid = __builtin_amdgcn_readfirstlane(tid >> 6);
    const unsigned lds0 = (unsigned)(uintptr_t)smem;

    unsigned voff[NI];
#pragma unroll
    for (int i = 0; i < NI; ++i) {
        const int id = wid * NI + i;
        if (id < NA) {
            int row = id * 8 + (lane >> 3);
            int ch = (lane & 7) ^ ((row >> 1) & 7);
            int gr = m0 + row; gr = gr > M_ - 1 ? M_ - 1 : gr;
            voff[i] = (unsigned)(gr * (K * 2) + ch * 16);
        } else {
            const int j = id - NA;
            int k, cb;
            if constexpr (BN == 128) { k = j * 4 + (lane >> 4); cb = ((lane & 15) >> 1) ^ ((k & 3) | (((k >> 3) & 1) << 2)); }
            else { k = j * 8 + (lane >> 3); cb = ((lane & 7) >> 1) ^ (((k >> 1) & 1) | (((k >> 3) & 1) << 1)); }
            int col = n0 + cb * 16 + (lane & 1) * 8; col = col > N - 8 ? N - 8 : col;
            voff[i] = (unsigned)(k * (N * 2) + col * 2);
        }
    }
    const char* pa = (const char*)A;
    const char* pb = (const char*)B;
    unsigned slot_i = 0;
    auto issue = [&]() {
#pragma unroll
        for (int i = 0; i < NI; ++i) {
            const int id = wid * NI + i;
            glds16(id < NA ? pa : pb, voff[i], lds0 + slot_i + id * 1024);
        }
        pa += 128; pb += 64 * N * 2;
        slot_i += ST; if (slot_i == NS * ST) slot_i = 0;
    };

    const int r = lane & 15, g = lane >> 4, q = (lane & 15) >> 2, p = lane & 3;
    const int wm = wid >> 1, wn = wid & 1;
    lds_cptr xa[2];
    {
        const int row = wm * WM + r;
#pragma unroll
        for (int s = 0; s < 2; ++s) xa[s] = (lds_cptr)smem + row * 128 + (((4 * s + g) ^ ((row >> 1) & 7)) * 16);
    }
    lds_cptr wa[2][TN];
#pragma unroll
    for (int t = 0; t < 2; ++t)
#pragma unroll
        for (int nt = 0; nt < TN; ++nt) {
            const int k = 8 * g + 4 * t + q, cb = wn * TN + nt;
            int sl;
            if constexpr (BN == 128) sl = cb ^ ((k & 3) | (((k >> 3) & 1) << 2));
            else sl = cb ^ (((k >> 1) & 1) | (((k >> 3) & 1) << 1));
            wa[t][nt] = (lds_cptr)smem + AB + k * (BN * 2) + sl * 32 + p * 8;
        }

    f32x4 acc[TM][TN];
#pragma unroll
    for (int mt = 0; mt < TM; ++mt)
#pragma unroll
        for (int nt = 0; nt < TN; ++nt) acc[mt][nt] = (f32x4){0.f, 0.f, 0.f, 0.f};

    bf16x8 af[2][TM], bf[2][TN];
    auto ldfrag = [&](auto BUF, auto S, unsigned so) {
        constexpr int buf = BUF.value, s = S.value;
#pragma unroll
        for (int mt = 0; mt < TM; ++mt) af[buf][mt] = lds_b128(xa[s] + so + mt * 2048);
#pragma unroll
        for (int nt = 0; nt < TN; ++nt)
            bf[buf][nt] = cat8(lds_tr(wa[0][nt] + so + s * 32 * BN * 2), lds_tr(wa[1][nt] + so + s * 32 * BN * 2));
    };
    auto mma = [&](auto BUF) {
        constexpr int buf = BUF.value;
#pragma unroll
        for (int nt = 0; nt < TN; ++nt)
#pragma unroll
            for (int mt = 0; mt < TM; ++mt) acc[mt][nt] = MFMA16(bf[buf][nt], af[buf][mt], acc[mt][nt]);
    };
    using I0 = std::integral_constant<int, 0>;
    using I1 = std::integral_constant<int, 1>;

#pragma unroll 1
    for (int i = 0; i < NS - 1; ++i) issue();
    wait_vm<NI*(NS - 2)>();
    __builtin_amdgcn_s_barrier();
    unsigned so = 0;
    ldfrag(I0{}, I0{}, so);
#pragma unroll 1
    for (int kt = 0; kt < NK; ++kt) {
        ldfrag(I1{}, I1{}, so);
        mma(I0{});
        if (kt + 1 < NK) {
            const int rem = NK - 2 - kt;
            if (rem >= NS - 3) wait_vm<NI*(NS - 3)>();
            else static_for<0, NS - 3>([&](auto J) { if (rem == J.value) wait_vm<NI * J.value>(); });
            __builtin_amdgcn_s_barrier();
            if (kt + NS - 1 < NK) issue();
            so += ST; if (so == NS * ST) so = 0;
            ldfrag(I0{}, I0{}, so);
        }
        mma(I1{});
    }

#pragma unroll
    for (int mt = 0; mt < TM; ++mt) {
        const int row = m0 + wm * WM + mt * 16 + r;
        u16* crow = C + (size_t)row * N + n0 + wn * WN + 4 * g;
#pragma unroll
        for (int nt = 0; nt < TN; ++nt) {
            if (row < M_ && n0 + wn * WN + nt * 16 < N) {
                f32x4 v = acc[mt][nt];
                u32x2 o;
                if constexpr (SILU) {
                    o.x = pack2(silu_f(bf16_round(v[0])), silu_f(bf16_round(v[1])));
                    o.y = pack2(silu_f(bf16_round(v[2])), silu_f(bf16_round(v[3])));
                } else {
                    o.x = pack2(v[0], v[1]);
                    o.y = pack2(v[2], v[3]);
                }
                *(u32x2*)(crow + nt * 16) = o;
            }
        }
    }
}

constexpr int G1_NS = 5;
__global__ __launch_bounds__(256, 1) void gemm1_kernel(const u16* __restrict__ x, const u16* __restrict__ w1, u16* __restrict__ h) {
    __shared__ __attribute__((aligned(128))) char smem[G1_NS * 32768];
    const int b = blockIdx.x, xcd = b & 7, idx = b >> 3;
    const int mb = (xcd >> 1) * 4 + (idx & 3), nb = (xcd & 1) * 7 + (idx >> 2);
    gemm_tile<128, 128, D_, F_, G1_NS, true>(x, w1, h, smem, mb * 128, nb * 128);
}

constexpr int G2_NS = 8;
__global__ __launch_bounds__(256, 1) void gemm2_kernel(const u16* __restrict__ h, const u16* __restrict__ w2, u16* __restrict__ out) {
    __shared__ __attribute__((aligned(128))) char smem[G2_NS * 20480];
    const int b = blockIdx.x, xcd = b & 7, idx = b >> 3;
    const int t = xcd * 28 + idx;
    if (t >= 220) return;
    const int mb = t / 10, nb = t - mb * 10;
    gemm_tile<96, 64, F_, D_, G2_NS, false>(h, w2, out, smem, mb * 96, nb * 64);
}

extern "C" void kernel_launch(void* const* d_in, const int* in_sizes, int n_in, void* d_out, int out_size, void* d_ws,
                              size_t ws_size, hipStream_t stream) {
    const u16* x = (const u16*)d_in[0];
    const u16* w1 = (const u16*)d_in[1];
    const u16* w2 = (const u16*)d_in[2];
    u16* h = (u16*)d_ws;
    u16* out = (u16*)d_out;
    gemm1_kernel<<<224, 256, 0, stream>>>(x, w1, h);
    gemm2_kernel<<<224, 256, 0, stream>>>(h, w2, out);
}
```

```cpp
#include <hip/hip_runtime.h>
#include <hip/hip_bf16.h>
#include <stdint.h>
#include <type_traits>

typedef unsigned short u16;
typedef __attribute__((ext_vector_type(8))) short bf16x8;
typedef __attribute__((ext_vector_type(4))) short s16x4;
typedef __attribute__((ext_vector_type(4))) float f32x4;
typedef __attribute__((ext_vector_type(2))) unsigned u32x2;
typedef __attribute__((address_space(3))) const char* lds_cptr;

constexpr int M_ = 2048, D_ = 640, F_ = 1728;

template <int I, int N, class Fn>
__device__ __forceinline__ void static_for(Fn&& f) {
    if constexpr (I < N) { f(std::integral_constant<int, I>{}); static_for<I + 1, N>(f); }
}
template <int N> __device__ __forceinline__ void wait_vm() { asm volatile("s_waitcnt vmcnt(%0)" ::"n"(N) : "memory"); }

__device__ __forceinline__ void glds16(const void* sbase, unsigned voff, unsigned lds_base) {
    unsigned keep;
    asm volatile("s_mov_b32 %0, m0\n\ts_mov_b32 m0, %3\n\ts_nop 0\n\tglobal_load_lds_dwordx4 %1, %2\n\ts_mov_b32 m0, %0"
                 : "=&s"(keep) : "v"(voff), "s"(sbase), "s"(lds_base) : "memory");
}
__device__ __forceinline__ bf16x8 lds_b128(lds_cptr p) { return *(const __attribute__((address_space(3))) bf16x8*)p; }
__device__ __forceinline__ s16x4 lds_tr(lds_cptr p) {
    return __builtin_amdgcn_ds_read_tr16_b64_v4i16((__attribute__((address_space(3))) s16x4*)p);
}
__device__ __forceinline__ void mfma16(f32x4& c, bf16x8 a, bf16x8 b) {
    asm volatile("v_mfma_f32_16x16x32_bf16 %0, %1, %2, %0" : "+a"(c) : "v"(a), "v"(b));
}
__device__ __forceinline__ float bf16_round(float v) { return (float)(__bf16)v; }
__device__ __forceinline__ unsigned pack2(float lo, float hi) {
    typedef __attribute__((ext_vector_type(2))) float f32x2;
    typedef __attribute__((ext_vector_type(2))) __bf16 bf16x2;
    f32x2 v = {lo, hi};
    bf16x2 r = __builtin_convertvector(v, bf16x2);
    return __builtin_bit_cast(unsigned, r);
}
__device__ __forceinline__ float silu_f(float v) { return v * __builtin_amdgcn_rcpf(1.0f + __expf(-v)); }

template <int BM, int BN, int K, int N, int NS, bool SILU>
__device__ __forceinline__ void gemm_tile(const u16* __restrict__ A, const u16* __restrict__ B, u16* __restrict__ C,
                                          char* smem, int m0, int n0, int kshift) {
    constexpr int NK = K / 64;
    constexpr int AB = BM * 128, BB = 64 * BN * 2, ST = AB + BB;
    constexpr int PA = BM / 32, PB = BB / 4096, NI = PA + PB;
    constexpr int WM = BM / 2, WN = BN / 2, TM = WM / 16, TN = WN / 16;
    const int tid = threadIdx.x, lane = tid & 63;
    const int wid = __builtin_amdgcn_readfirstlane(tid >> 6);
    if (wid >= 4) {
        const int pw = wid - 4;
        unsigned voff[NI];
#pragma unroll
        for (int i = 0; i < PA; ++i) {
            int row = (pw * PA + i) * 8 + (lane >> 3);
            int ch = (lane & 7) ^ ((row >> 1) & 7);
            int gr = m0 + row; gr = gr > M_ - 1 ? M_ - 1 : gr;
            voff[i] = (unsigned)(gr * (K * 2) + ch * 16);
        }
#pragma unroll
        for (int i = 0; i < PB; ++i) {
            const int j = pw * PB + i;
            int k, cb;
            if constexpr (BN == 128) { k = j * 4 + (lane >> 4); cb = ((lane & 15) >> 1) ^ ((k & 3) | (((k >> 3) & 1) << 2)); }
            else { k = j * 8 + (lane >> 3); cb = ((lane & 7) >> 1) ^ (((k >> 1) & 1) | (((k >> 3) & 1) << 1)); }
            int col = n0 + cb * 16 + (lane & 1) * 8; col = col > N - 8 ? N - 8 : col;
            voff[PA + i] = (unsigned)(k * (N * 2) + col * 2);
        }
        const char* pa = (const char*)A + kshift * 128;
        const char* pb = (const char*)B + (size_t)kshift * (64 * N * 2);
        int kcur = kshift;
        const unsigned da = (unsigned)(uintptr_t)smem + pw * PA * 1024, db = (unsigned)(uintptr_t)smem + AB + pw * PB * 1024;
        unsigned slot = 0;
        auto issue = [&]() {
#pragma unroll
            for (int i = 0; i < PA; ++i) glds16(pa, voff[i], da + slot + i * 1024);
#pragma unroll
            for (int i = 0; i < PB; ++i) glds16(pb, voff[PA + i], db + slot + i * 1024);
            pa += 128; pb += 64 * N * 2;
            if (++kcur == NK) { kcur = 0; pa -= NK * 128; pb -= (size_t)NK * (64 * N * 2); }
            slot += ST; if (slot == NS * ST) slot = 0;
        };
        issue(); issue();
        wait_vm<NI>();
        __builtin_amdgcn_s_barrier();
#pragma unroll 1
        for (int i = 2; i < NS - 1; ++i) issue();
#pragma unroll 1
        for (int kt = 0; kt < NK - NS + 1; ++kt) {
            wait_vm<NI*(NS - 3)>();
            __builtin_amdgcn_s_barrier();
            issue();
        }
        static_for<0, NS - 2>([&](auto J) {
            wait_vm<NI*(NS - 3 - J.value)>();
            __builtin_amdgcn_s_barrier();
        });
        if constexpr (!SILU) return;
    } else {
    const int r = lane & 15, g = lane >> 4, q = (lane & 15) >> 2, p = lane & 3;
    const int wm = wid >> 1, wn = wid & 1;
    lds_cptr xa[2];
    {
        const int row = wm * WM + r;
#pragma unroll
        for (int s = 0; s < 2; ++s) xa[s] = (lds_cptr)smem + row * 128 + (((4 * s + g) ^ ((row >> 1) & 7)) * 16);
    }
    lds_cptr wa[2][TN];
#pragma unroll
    for (int t = 0; t < 2; ++t)
#pragma unroll
        for (int nt = 0; nt < TN; ++nt) {
            const int k = 8 * g + 4 * t + q, cb = wn * TN + nt;
            int sl;
            if constexpr (BN == 128) sl = cb ^ ((k & 3) | (((k >> 3) & 1) << 2));
            else sl = cb ^ (((k >> 1) & 1) | (((k >> 3) & 1) << 1));
            wa[t][nt] = (lds_cptr)smem + AB + k * (BN * 2) + sl * 32 + p * 8;
        }
    f32x4 acc[TM][TN];
#pragma unroll
    for (int mt = 0; mt < TM; ++mt)
#pragma unroll
        for (int nt = 0; nt < TN; ++nt) acc[mt][nt] = (f32x4){0.f, 0.f, 0.f, 0.f};

    bf16x8 af[2][TM];
    s16x4 bl[2][TN], bh[2][TN];
    constexpr int NR = TM + 2 * TN, NM = TM * TN;
    auto rd = [&](auto BUF, auto S, auto J, unsigned so) {
        constexpr int buf = BUF.value, s = S.value, j = J.value;
        if constexpr (j >= 2 && j < 2 + TM) af[buf][j - 2] = lds_b128(xa[s] + so + (j - 2) * 2048);
        else {
            constexpr int jb = j < 2 ? j : j - TM;
            if constexpr ((jb & 1) == 0) bl[buf][jb >> 1] = lds_tr(wa[0][jb >> 1] + so + s * 32 * BN * 2);
            else bh[buf][jb >> 1] = lds_tr(wa[1][jb >> 1] + so + s * 32 * BN * 2);
        }
    };
    auto phase = [&](auto BUF, auto SN, unsigned so, auto DORD) {
        constexpr int buf = BUF.value;
        static_for<0, NM>([&](auto J) {
            constexpr int j = J.value, nt = j / TM, mt = j % TM;
            mfma16(acc[mt][nt], __builtin_shufflevector(bl[buf][nt], bh[buf][nt], 0, 1, 2, 3, 4, 5, 6, 7), af[buf][mt]);
            if constexpr (DORD.value) {
                constexpr int NS_ = NM - NM / 4;
                constexpr int lo = (j < NS_ ? j : NS_) * NR / NS_, hi = (j + 1 < NS_ ? j + 1 : NS_) * NR / NS_;
                static_for<lo, hi>([&](auto R) { rd(std::integral_constant<int, buf ^ 1>{}, SN, R, so); });
            }
            __builtin_amdgcn_sched_barrier(0);
        });
    };
    using I0 = std::integral_constant<int, 0>;
    using I1 = std::integral_constant<int, 1>;
    __builtin_amdgcn_s_barrier();
    unsigned so = 0;
    static_for<0, NR>([&](auto R) { rd(I0{}, I0{}, R, 0u); });
#pragma unroll 1
    for (int kt = 0; kt < NK - 1; ++kt) {
        phase(I0{}, I1{}, so, I1{});
        __builtin_amdgcn_s_barrier();
        so += ST; if (so == NS * ST) so = 0;
        phase(I1{}, I0{}, so, I1{});
    }
    phase(I0{}, I1{}, so, I1{});
    phase(I1{}, I0{}, so, I0{});

    asm volatile("s_nop 15\n\ts_nop 15" ::: "memory");
    if constexpr (!SILU) {
#pragma unroll
        for (int mt = 0; mt < TM; ++mt) {
            const int row = m0 + wm * WM + mt * 16 + r;
            u16* crow = C + (size_t)row * N + n0 + wn * WN + 4 * g;
#pragma unroll
            for (int nt = 0; nt < TN; ++nt) {
                if (row < M_ && n0 + wn * WN + nt * 16 < N) {
                    f32x4 v = acc[mt][nt];
                    u32x2 o;
                    o.x = pack2(v[0], v[1]);
                    o.y = pack2(v[2], v[3]);
                    *(u32x2*)(crow + nt * 16) = o;
                }
            }
        }
        return;
    } else {
        static_assert(!SILU || ((NK - 1) % NS != 0 && (NK - 2) % NS != 0 && BM * BN * 2 <= ST && BN == 128), "epilogue slot");
#pragma unroll
        for (int mt = 0; mt < TM; ++mt)
#pragma unroll
            for (int nt = 0; nt < TN; ++nt) {
                f32x4 v = acc[mt][nt];
                u32x2 o;
                o.x = pack2(v[0], v[1]);
                o.y = pack2(v[2], v[3]);
                const int row = wm * WM + mt * 16 + r, c = (wn * WN + nt * 16) / 8 + (g >> 1);
                *(u32x2*)(smem + row * 256 + ((c ^ r) * 16) + (g & 1) * 8) = o;
            }
    }
    }
    if constexpr (SILU) {
        asm volatile("s_waitcnt lgkmcnt(0)" ::: "memory");
        __builtin_amdgcn_s_barrier();
        const int c = lane & 15;
#pragma unroll
        for (int i = 0; i < BM / 32; ++i) {
            const int row = wid * (BM / 8) + i * 4 + (lane >> 4);
            typedef __attribute__((ext_vector_type(4))) unsigned u32x4;
            u32x4 v = *(const u32x4*)(smem + row * 256 + ((c ^ (row & 15)) * 16));
            u32x4 o;
#pragma unroll
            for (int e = 0; e < 4; ++e)
                o[e] = pack2(silu_f(__uint_as_float(v[e] << 16)), silu_f(__uint_as_float(v[e] & 0xffff0000u)));
            if (n0 + c * 8 < N) *(u32x4*)(C + (size_t)(m0 + row) * N + n0 + c * 8) = o;
        }
        if (wid < 4) {   }
    }
}

constexpr int G1_NS = 5;
__global__ __launch_bounds__(512, 2) void gemm1_kernel(const u16* __restrict__ x, const u16* __restrict__ w1, u16* __restrict__ h) {
    __shared__ __attribute__((aligned(128))) char smem[G1_NS * 32768];
    const int b = blockIdx.x, xcd = b & 7, idx = b >> 3;
    const int mb = (xcd >> 1) * 4 + (idx & 3), nb = (xcd & 1) * 7 + (idx >> 2);
    gemm_tile<128, 128, D_, F_, G1_NS, true>(x, w1, h, smem, mb * 128, nb * 128, ((idx >> 2) + 3 * (idx & 3)) % 10);
}

constexpr int G2_NS = 8;
__global__ __launch_bounds__(512, 2) void gemm2_kernel(const u16* __restrict__ h, const u16* __restrict__ w2, u16* __restrict__ out) {
    __shared__ __attribute__((aligned(128))) char smem[G2_NS * 20480];
    const int b = blockIdx.x, xcd = b & 7, idx = b >> 3;
    const int t = xcd * 28 + idx;
    if (t >= 220) return;
    const int mb = t / 10, nb = t - mb * 10;
    gemm_tile<96, 64, F_, D_, G2_NS, false>(h, w2, out, smem, mb * 96, nb * 64, (nb * 27 / 10 + mb % 3) % 27);
}

extern "C" void kernel_launch(void* const* d_in, const int* in_sizes, int n_in, void* d_out, int out_size, void* d_ws,
                              size_t ws_size, hipStream_t stream) {
    const u16* x = (const u16*)d_in[0];
    const u16* w1 = (const u16*)d_in[1];
    const u16* w2 = (const u16*)d_in[2];
    u16* h = (u16*)d_ws;
    u16* out = (u16*)d_out;
    gemm1_kernel<<<224, 512, 0, stream>>>(x, w1, h);
    gemm2_kernel<<<224, 512, 0, stream>>>(h, w2, out);
}
```

```cpp
#include <hip/hip_runtime.h>
#include <hip/hip_bf16.h>
#include <stdint.h>
#include <type_traits>

typedef unsigned short u16;
typedef __attribute__((ext_vector_type(8))) short bf16x8;
typedef __attribute__((ext_vector_type(4))) short s16x4;
typedef __attribute__((ext_vector_type(4))) float f32x4;
typedef __attribute__((ext_vector_type(2))) unsigned u32x2;
typedef __attribute__((address_space(3))) const char* lds_cptr;

constexpr int M_ = 2048, D_ = 640, F_ = 1728;

template <int I, int N, class Fn>
__device__ __forceinline__ void static_for(Fn&& f) {
    if constexpr (I < N) { f(std::integral_constant<int, I>{}); static_for<I + 1, N>(f); }
}
template <int N> __device__ __forceinline__ void wait_vm() { asm volatile("s_waitcnt vmcnt(%0)" ::"n"(N) : "memory"); }

__device__ __forceinline__ void glds16(const void* sbase, unsigned voff, unsigned lds_base) {
    unsigned keep;
    asm volatile("s_mov_b32 %0, m0\n\ts_mov_b32 m0, %3\n\ts_nop 0\n\tglobal_load_lds_dwordx4 %1, %2\n\ts_mov_b32 m0, %0"
                 : "=&s"(keep) : "v"(voff), "s"(sbase), "s"(lds_base) : "memory");
}
__device__ __forceinline__ bf16x8 lds_b128(lds_cptr p) { return *(const __attribute__((address_space(3))) bf16x8*)p; }
__device__ __forceinline__ s16x4 lds_tr(lds_cptr p) {
    return __builtin_amdgcn_ds_read_tr16_b64_v4i16((__attribute__((address_space(3))) s16x4*)p);
}
__device__ __forceinline__ void mfma16(f32x4& c, bf16x8 a, bf16x8 b) {
    asm volatile("v_mfma_f32_16x16x32_bf16 %0, %1, %2, %0" : "+a"(c) : "v"(a), "v"(b));
}
__device__ __forceinline__ float bf16_round(float v) { return (float)(__bf16)v; }
__device__ __forceinline__ unsigned pack2(float lo, float hi) {
    typedef __attribute__((ext_vector_type(2))) float f32x2;
    typedef __attribute__((ext_vector_type(2))) __bf16 bf16x2;
    f32x2 v = {lo, hi};
    bf16x2 r = __builtin_convertvector(v, bf16x2);
    return __builtin_bit_cast(unsigned, r);
}
__device__ __forceinline__ float silu_f(float v) { return v * __builtin_amdgcn_rcpf(1.0f + __expf(-v)); }

template <int BM, int BN, int K, int N, int NS, bool SILU>
__device__ __forceinline__ void gemm_tile(const u16* __restrict__ A, const u16* __restrict__ B, u16* __restrict__ C,
                                          char* smem, int m0, int n0, int kshift) {
    constexpr int NK = K / 64;
    constexpr int AB = BM * 128, BB = 64 * BN * 2, ST = AB + BB;
    constexpr int PA = BM / 32, PB = BB / 4096, NI = PA + PB;
    constexpr int WM = BM / 2, WN = BN / 2, TM = WM / 16, TN = WN / 16;
    const int tid = threadIdx.x, lane = tid & 63;
    const int wid = __builtin_amdgcn_readfirstlane(tid >> 6);
    asm volatile("" ::"s"(A), "s"(B), "s"(C));
    if (wid >= 4) {
        const int pw = wid - 4;
        unsigned voff[NI];
#pragma unroll
        for (int i = 0; i < PA; ++i) {
            int row = (pw * PA + i) * 8 + (lane >> 3);
            int ch = (lane & 7) ^ ((row >> 1) & 7);
            int gr = m0 + row; gr = gr > M_ - 1 ? M_ - 1 : gr;
            voff[i] = (unsigned)(gr * (K * 2) + ch * 16);
        }
#pragma unroll
        for (int i = 0; i < PB; ++i) {
            const int j = pw * PB + i;
            int k, cb;
            if constexpr (BN == 128) { k = j * 4 + (lane >> 4); cb = ((lane & 15) >> 1) ^ ((k & 3) | (((k >> 3) & 1) << 2)); }
            else { k = j * 8 + (lane >> 3); cb = ((lane & 7) >> 1) ^ (((k >> 1) & 1) | (((k >> 3) & 1) << 1)); }
            int col = n0 + cb * 16 + (lane & 1) * 8; col = col > N - 8 ? N - 8 : col;
            voff[PA + i] = (unsigned)(k * (N * 2) + col * 2);
        }
        const char* pa = (const char*)A + kshift * 128;
        const char* pb = (const char*)B + (size_t)kshift * (64 * N * 2);
        int kcur = kshift;
        const unsigned da = (unsigned)(uintptr_t)smem + pw * PA * 1024, db = (unsigned)(uintptr_t)smem + AB + pw * PB * 1024;
        unsigned slot = 0;
        auto issue = [&]() {
#pragma unroll
            for (int i = 0; i < PA; ++i) glds16(pa, voff[i], da + slot + i * 1024);
#pragma unroll
            for (int i = 0; i < PB; ++i) glds16(pb, voff[PA + i], db + slot + i * 1024);
            pa += 128; pb += 64 * N * 2;
            if (++kcur == NK) { kcur = 0; pa -= NK * 128; pb -= (size_t)NK * (64 * N * 2); }
            slot += ST; if (slot == NS * ST) slot = 0;
        };
        issue(); issue();
        wait_vm<NI>();
        __builtin_amdgcn_s_barrier();
#pragma unroll 1
        for (int i = 2; i < NS - 1; ++i) issue();
#pragma unroll 1
        for (int kt = 0; kt < NK - NS + 1; ++kt) {
            wait_vm<NI*(NS - 3)>();
            __builtin_amdgcn_s_barrier();
            issue();
        }
        static_for<0, NS - 2>([&](auto J) {
            wait_vm<NI*(NS - 3 - J.value)>();
            __builtin_amdgcn_s_barrier();
        });
        if constexpr (!SILU) return;
    } else {
    const int r = lane & 15, g = lane >> 4, q = (lane & 15) >> 2, p = lane & 3;
    const int wm = wid >> 1, wn = wid & 1;
    lds_cptr xa[2];
    {
        const int row = wm * WM + r;
#pragma unroll
        for (int s = 0; s < 2; ++s) xa[s] = (lds_cptr)smem + row * 128 + (((4 * s + g) ^ ((row >> 1) & 7)) * 16);
    }
    lds_cptr wa[2][TN];
#pragma unroll
    for (int t = 0; t < 2; ++t)
#pragma unroll
        for (int nt = 0; nt < TN; ++nt) {
            const int k = 8 * g + 4 * t + q, cb = wn * TN + nt;
            int sl;
            if constexpr (BN == 128) sl = cb ^ ((k & 3) | (((k >> 3) & 1) << 2));
            else sl = cb ^ (((k >> 1) & 1) | (((k >> 3) & 1) << 1));
            wa[t][nt] = (lds_cptr)smem + AB + k * (BN * 2) + sl * 32 + p * 8;
        }
    f32x4 acc[TM][TN];
#pragma unroll
    for (int mt = 0; mt < TM; ++mt)
#pragma unroll
        for (int nt = 0; nt < TN; ++nt) acc[mt][nt] = (f32x4){0.f, 0.f, 0.f, 0.f};

    bf16x8 af[2][TM];
    s16x4 bl[2][TN], bh[2][TN];
    constexpr int NR = TM + 2 * TN, NM = TM * TN;
    auto rd = [&](auto BUF, auto S, auto J, unsigned so) {
        constexpr int buf = BUF.value, s = S.value, j = J.value;
        if constexpr (j >= 2 && j < 2 + TM) af[buf][j - 2] = lds_b128(xa[s] + so + (j - 2) * 2048);
        else {
            constexpr int jb = j < 2 ? j : j - TM;
            if constexpr ((jb & 1) == 0) bl[buf][jb >> 1] = lds_tr(wa[0][jb >> 1] + so + s * 32 * BN * 2);
            else bh[buf][jb >> 1] = lds_tr(wa[1][jb >> 1] + so + s * 32 * BN * 2);
        }
    };
    auto phase = [&](auto BUF, auto SN, unsigned so, auto DORD) {
        constexpr int buf = BUF.value;
        static_for<0, NM>([&](auto J) {
            constexpr int j = J.value, nt = j / TM, mt = j % TM;
            mfma16(acc[mt][nt], __builtin_shufflevector(bl[buf][nt], bh[buf][nt], 0, 1, 2, 3, 4, 5, 6, 7), af[buf][mt]);
            if constexpr (DORD.value) {
                constexpr int NS_ = NM - NM / 4;
                constexpr int lo = (j < NS_ ? j : NS_) * NR / NS_, hi = (j + 1 < NS_ ? j + 1 : NS_) * NR / NS_;
                static_for<lo, hi>([&](auto R) { rd(std::integral_constant<int, buf ^ 1>{}, SN, R, so); });
            }
            __builtin_amdgcn_sched_barrier(0);
        });
    };
    using I0 = std::integral_constant<int, 0>;
    using I1 = std::integral_constant<int, 1>;
    __builtin_amdgcn_s_barrier();
    unsigned so = 0;
    static_for<0, NR>([&](auto R) { rd(I0{}, I0{}, R, 0u); });
#pragma unroll 1
    for (int kt = 0; kt < NK - 1; ++kt) {
        phase(I0{}, I1{}, so, I1{});
        __builtin_amdgcn_s_barrier();
        so += ST; if (so == NS * ST) so = 0;
        phase(I1{}, I0{}, so, I1{});
    }
    phase(I0{}, I1{}, so, I1{});
    phase(I1{}, I0{}, so, I0{});

    asm volatile("s_nop 15\n\ts_nop 15" ::: "memory");
    if constexpr (!SILU) {
#pragma unroll
        for (int mt = 0; mt < TM; ++mt) {
            const int row = m0 + wm * WM + mt * 16 + r;
            u16* crow = C + (size_t)row * N + n0 + wn * WN + 4 * g;
#pragma unroll
            for (int nt = 0; nt < TN; ++nt) {
                if (row < M_ && n0 + wn * WN + nt * 16 < N) {
                    f32x4 v = acc[mt][nt];
                    u32x2 o;
                    o.x = pack2(v[0], v[1]);
                    o.y = pack2(v[2], v[3]);
                    *(u32x2*)(crow + nt * 16) = o;
                }
            }
        }
        return;
    } else {
        static_assert(!SILU || ((NK - 1) % NS != 0 && (NK - 2) % NS != 0 && BM * BN * 2 <= ST && BN == 128), "epilogue slot");
#pragma unroll
        for (int mt = 0; mt < TM; ++mt)
#pragma unroll
            for (int nt = 0; nt < TN; ++nt) {
                f32x4 v = acc[mt][nt];
                u32x2 o;
                o.x = pack2(v[0], v[1]);
                o.y = pack2(v[2], v[3]);
                const int row = wm * WM + mt * 16 + r, c = (wn * WN + nt * 16) / 8 + (g >> 1);
                *(u32x2*)(smem + row * 256 + ((c ^ r) * 16) + (g & 1) * 8) = o;
            }
    }
    }
    if constexpr (SILU) {
        asm volatile("s_waitcnt lgkmcnt(0)" ::: "memory");
        __builtin_amdgcn_s_barrier();
        const int c = lane & 15;
#pragma unroll
        for (int i = 0; i < BM / 32; ++i) {
            const int row = wid * (BM / 8) + i * 4 + (lane >> 4);
            typedef __attribute__((ext_vector_type(4))) unsigned u32x4;
            u32x4 v = *(const u32x4*)(smem + row * 256 + ((c ^ (row & 15)) * 16));
            u32x4 o;
#pragma unroll
            for (int e = 0; e < 4; ++e)
                o[e] = pack2(silu_f(__uint_as_float(v[e] << 16)), silu_f(__uint_as_float(v[e] & 0xffff0000u)));
            if (n0 + c * 8 < N) *(u32x4*)(C + (size_t)(m0 + row) * N + n0 + c * 8) = o;
        }
        if (wid < 4) {   }
    }
}

constexpr int G1_NS = 5;
__global__ __launch_bounds__(512, 2) void gemm1_kernel(const u16* __restrict__ x, const u16* __restrict__ w1, u16* __restrict__ h,
                                                       const u16* __restrict__ w2) {
    __shared__ __attribute__((aligned(128))) char smem[G1_NS * 32768];
    if (blockIdx.x >= 224) {
        const int w = __builtin_amdgcn_readfirstlane(threadIdx.x >> 6);
        const char* p = (const char*)w2 + (size_t)(blockIdx.x - 224) * (F_ * D_ * 2 / 32) + w * 8640;
        const unsigned dst = (unsigned)(uintptr_t)smem + w * 1024;
        const unsigned vo = (threadIdx.x & 63) * 16;
#pragma unroll 1
        for (int i = 0; i < 8; ++i) glds16(p + i * 1024, vo, dst);
        if ((threadIdx.x & 63) < 28) glds16(p + 8192, vo, dst);
        wait_vm<0>();
        return;
    }
    const int b = blockIdx.x, xcd = b & 7, idx = b >> 3;
    const int mb = (xcd >> 1) * 4 + (idx & 3), nb = (xcd & 1) * 7 + (idx >> 2);
    gemm_tile<128, 128, D_, F_, G1_NS, true>(x, w1, h, smem, mb * 128, nb * 128, ((idx >> 2) + 3 * (idx & 3)) % 10);
}

constexpr int G2_NS = 8;
__global__ __launch_bounds__(512, 2) void gemm2_kernel(const u16* __restrict__ h, const u16* __restrict__ w2, u16* __restrict__ out) {
    __shared__ __attribute__((aligned(128))) char smem[G2_NS * 20480];
    const int b = blockIdx.x, xcd = b & 7, idx = b >> 3;
    const int t = xcd * 28 + idx;
    if (t >= 220) return;
    const int mb = t / 10, nb = t - mb * 10;
    gemm_tile<96, 64, F_, D_, G2_NS, false>(h, w2, out, smem, mb * 96, nb * 64, (nb * 27 / 10 + (mb % 3) * 9) % 27);
}

extern "C" void kernel_launch(void* const* d_in, const int* in_sizes, int n_in, void* d_out, int out_size, void* d_ws,
                              size_t ws_size, hipStream_t stream) {
    const u16* x = (const u16*)d_in[0];
    const u16* w1 = (const u16*)d_in[1];
    const u16* w2 = (const u16*)d_in[2];
    u16* h = (u16*)d_ws;
    u16* out = (u16*)d_out;
    gemm1_kernel<<<256, 512, 0, stream>>>(x, w1, h, w2);
    gemm2_kernel<<<224, 512, 0, stream>>>(h, w2, out);
}
```

```cpp
#include <hip/hip_runtime.h>
#include <hip/hip_bf16.h>
#include <stdint.h>
#include <type_traits>

typedef unsigned short u16;
typedef __attribute__((ext_vector_type(8))) short bf16x8;
typedef __attribute__((ext_vector_type(4))) short s16x4;
typedef __attribute__((ext_vector_type(4))) float f32x4;
typedef __attribute__((ext_vector_type(2))) unsigned u32x2;
typedef __attribute__((address_space(3))) const char* lds_cptr;

constexpr int M_ = 2048, D_ = 640, F_ = 1728;

template <int I, int N, class Fn>
__device__ __forceinline__ void static_for(Fn&& f) {
    if constexpr (I < N) { f(std::integral_constant<int, I>{}); static_for<I + 1, N>(f); }
}
template <int N> __device__ __forceinline__ void wait_vm() { asm volatile("s_waitcnt vmcnt(%0)" ::"n"(N) : "memory"); }

__device__ __forceinline__ void glds16(const void* sbase, unsigned voff, unsigned lds_base) {
    unsigned keep;
    asm volatile("s_mov_b32 %0, m0\n\ts_mov_b32 m0, %3\n\ts_nop 0\n\tglobal_load_lds_dwordx4 %1, %2\n\ts_mov_b32 m0, %0"
                 : "=&s"(keep) : "v"(voff), "s"(sbase), "s"(lds_base) : "memory");
}
__device__ __forceinline__ bf16x8 lds_b128(lds_cptr p) { return *(const __attribute__((address_space(3))) bf16x8*)p; }
__device__ __forceinline__ s16x4 lds_tr(lds_cptr p) {
    return __builtin_amdgcn_ds_read_tr16_b64_v4i16((__attribute__((address_space(3))) s16x4*)p);
}
__device__ __forceinline__ void mfma16(f32x4& c, bf16x8 a, bf16x8 b) {
    asm volatile("v_mfma_f32_16x16x32_bf16 %0, %1, %2, %0" : "+a"(c) : "v"(a), "v"(b));
}
__device__ __forceinline__ float bf16_round(float v) { return (float)(__bf16)v; }
__device__ __forceinline__ unsigned pack2(float lo, float hi) {
    typedef __attribute__((ext_vector_type(2))) float f32x2;
    typedef __attribute__((ext_vector_type(2))) __bf16 bf16x2;
    f32x2 v = {lo, hi};
    bf16x2 r = __builtin_convertvector(v, bf16x2);
    return __builtin_bit_cast(unsigned, r);
}
__device__ __forceinline__ float silu_f(float v) { return v * __builtin_amdgcn_rcpf(1.0f + __expf(-v)); }

template <int BM, int BN, int K, int N, int NS, bool SILU>
__device__ __forceinline__ void gemm_tile(const u16* __restrict__ A, const u16* __restrict__ B, u16* __restrict__ C,
                                          char* smem, int m0, int n0, int kshift,
                                          const char* pfa, int pfa_lines, const char* pfb, int slice) {
    constexpr int NK = K / 64;
    constexpr int AB = BM * 128, BB = 64 * BN * 2, ST = AB + BB;
    constexpr int PA = BM / 32, PB = BB / 4096, NI = PA + PB;
    constexpr int WM = BM / 2, WN = BN / 2, TM = WM / 16, TN = WN / 16;
    const int tid = threadIdx.x, lane = tid & 63;
    const int wid = __builtin_amdgcn_readfirstlane(tid >> 6);
    asm volatile("" ::"s"(A), "s"(B), "s"(C));
    if (wid >= 4) {
        const int pw = wid - 4;
        unsigned voff[NI];
#pragma unroll
        for (int i = 0; i < PA; ++i) {
            int row = (pw * PA + i) * 8 + (lane >> 3);
            int ch = (lane & 7) ^ ((row >> 1) & 7);
            int gr = m0 + row; gr = gr > M_ - 1 ? M_ - 1 : gr;
            voff[i] = (unsigned)(gr * (K * 2) + ch * 16);
        }
#pragma unroll
        for (int i = 0; i < PB; ++i) {
            const int j = pw * PB + i;
            int k, cb;
            if constexpr (BN == 128) { k = j * 4 + (lane >> 4); cb = ((lane & 15) >> 1) ^ ((k & 3) | (((k >> 3) & 1) << 2)); }
            else { k = j * 8 + (lane >> 3); cb = ((lane & 7) >> 1) ^ (((k >> 1) & 1) | (((k >> 3) & 1) << 1)); }
            int col = n0 + cb * 16 + (lane & 1) * 8; col = col > N - 8 ? N - 8 : col;
            voff[PA + i] = (unsigned)(k * (N * 2) + col * 2);
        }
        const char* pa = (const char*)A + kshift * 128;
        const char* pb = (const char*)B + (size_t)kshift * (64 * N * 2);
        int kcur = kshift;
        const unsigned da = (unsigned)(uintptr_t)smem + pw * PA * 1024, db = (unsigned)(uintptr_t)smem + AB + pw * PB * 1024;
        unsigned slot = 0;
        auto issue = [&]() {
#pragma unroll
            for (int i = 0; i < PA; ++i) glds16(pa, voff[i], da + slot + i * 1024);
#pragma unroll
            for (int i = 0; i < PB; ++i) glds16(pb, voff[PA + i], db + slot + i * 1024);
            pa += 128; pb += 64 * N * 2;
            if (++kcur == NK) { kcur = 0; pa -= NK * 128; pb -= (size_t)NK * (64 * N * 2); }
            slot += ST; if (slot == NS * ST) slot = 0;
        };
        issue(); issue();
        wait_vm<NI>();
        __builtin_amdgcn_s_barrier();
#pragma unroll 1
        for (int i = 2; i < NS - 1; ++i) issue();
#pragma unroll 1
        for (int kt = 0; kt < NK - NS + 1; ++kt) {
            wait_vm<NI*(NS - 3)>();
            __builtin_amdgcn_s_barrier();
            issue();
        }
        static_for<0, NS - 2>([&](auto J) {
            wait_vm<NI*(NS - 3 - J.value)>();
            __builtin_amdgcn_s_barrier();
        });
        if constexpr (!SILU) return;
    } else {
    constexpr int PFLB = (BN == 128) ? 14 : 10, PFB_LINES = K * PFLB;
    constexpr int NPF = (BN == 128) ? 2 : 4;
    unsigned pfd[NPF];
    for (int i = 0; i < NPF; ++i) pfd[i] = 0;
    const int r = lane & 15, g = lane >> 4, q = (lane & 15) >> 2, p = lane & 3;
    const int wm = wid >> 1, wn = wid & 1;
    lds_cptr xa[2];
    {
        const int row = wm * WM + r;
#pragma unroll
        for (int s = 0; s < 2; ++s) xa[s] = (lds_cptr)smem + row * 128 + (((4 * s + g) ^ ((row >> 1) & 7)) * 16);
    }
    lds_cptr wa[2][TN];
#pragma unroll
    for (int t = 0; t < 2; ++t)
#pragma unroll
        for (int nt = 0; nt < TN; ++nt) {
            const int k = 8 * g + 4 * t + q, cb = wn * TN + nt;
            int sl;
            if constexpr (BN == 128) sl = cb ^ ((k & 3) | (((k >> 3) & 1) << 2));
            else sl = cb ^ (((k >> 1) & 1) | (((k >> 3) & 1) << 1));
            wa[t][nt] = (lds_cptr)smem + AB + k * (BN * 2) + sl * 32 + p * 8;
        }
    f32x4 acc[TM][TN];
#pragma unroll
    for (int mt = 0; mt < TM; ++mt)
#pragma unroll
        for (int nt = 0; nt < TN; ++nt) acc[mt][nt] = (f32x4){0.f, 0.f, 0.f, 0.f};

    bf16x8 af[2][TM];
    s16x4 bl[2][TN], bh[2][TN];
    constexpr int NR = TM + 2 * TN, NM = TM * TN;
    auto rd = [&](auto BUF, auto S, auto J, unsigned so) {
        constexpr int buf = BUF.value, s = S.value, j = J.value;
        if constexpr (j >= 2 && j < 2 + TM) af[buf][j - 2] = lds_b128(xa[s] + so + (j - 2) * 2048);
        else {
            constexpr int jb = j < 2 ? j : j - TM;
            if constexpr ((jb & 1) == 0) bl[buf][jb >> 1] = lds_tr(wa[0][jb >> 1] + so + s * 32 * BN * 2);
            else bh[buf][jb >> 1] = lds_tr(wa[1][jb >> 1] + so + s * 32 * BN * 2);
        }
    };
    auto phase = [&](auto BUF, auto SN, unsigned so, auto DORD) {
        constexpr int buf = BUF.value;
        static_for<0, NM>([&](auto J) {
            constexpr int j = J.value, nt = j / TM, mt = j % TM;
            mfma16(acc[mt][nt], __builtin_shufflevector(bl[buf][nt], bh[buf][nt], 0, 1, 2, 3, 4, 5, 6, 7), af[buf][mt]);
            if constexpr (DORD.value) {
                constexpr int NS_ = NM - NM / 4;
                constexpr int lo = (j < NS_ ? j : NS_) * NR / NS_, hi = (j + 1 < NS_ ? j + 1 : NS_) * NR / NS_;
                static_for<lo, hi>([&](auto R) { rd(std::integral_constant<int, buf ^ 1>{}, SN, R, so); });
            }
            __builtin_amdgcn_sched_barrier(0);
        });
    };
    using I0 = std::integral_constant<int, 0>;
    using I1 = std::integral_constant<int, 1>;
    __builtin_amdgcn_s_barrier();
    if constexpr (NPF == 2) asm volatile("s_waitcnt vmcnt(0)" ::"v"(pfd[0]), "v"(pfd[1]) : "memory");
    else asm volatile("s_waitcnt vmcnt(0)" ::"v"(pfd[0]), "v"(pfd[1]), "v"(pfd[2]), "v"(pfd[3]) : "memory");
    unsigned so = 0;
    static_for<0, NR>([&](auto R) { rd(I0{}, I0{}, R, 0u); });
#pragma unroll 1
    for (int kt = 0; kt < NK - 1; ++kt) {
        phase(I0{}, I1{}, so, I1{});
        __builtin_amdgcn_s_barrier();
        so += ST; if (so == NS * ST) so = 0;
        phase(I1{}, I0{}, so, I1{});
    }
    phase(I0{}, I1{}, so, I1{});
    phase(I1{}, I0{}, so, I0{});

    asm volatile("s_nop 15\n\ts_nop 15" ::: "memory");
    if constexpr (!SILU) {
#pragma unroll
        for (int mt = 0; mt < TM; ++mt) {
            const int row = m0 + wm * WM + mt * 16 + r;
            u16* crow = C + (size_t)row * N + n0 + wn * WN + 4 * g;
#pragma unroll
            for (int nt = 0; nt < TN; ++nt) {
                if (row < M_ && n0 + wn * WN + nt * 16 < N) {
                    f32x4 v = acc[mt][nt];
                    u32x2 o;
                    o.x = pack2(v[0], v[1]);
                    o.y = pack2(v[2], v[3]);
                    *(u32x2*)(crow + nt * 16) = o;
                }
            }
        }
        return;
    } else {
        static_assert(!SILU || ((NK - 1) % NS != 0 && (NK - 2) % NS != 0 && BM * BN * 2 <= ST && BN == 128), "epilogue slot");
#pragma unroll
        for (int mt = 0; mt < TM; ++mt)
#pragma unroll
            for (int nt = 0; nt < TN; ++nt) {
                f32x4 v = acc[mt][nt];
                u32x2 o;
                o.x = pack2(v[0], v[1]);
                o.y = pack2(v[2], v[3]);
                const int row = wm * WM + mt * 16 + r, c = (wn * WN + nt * 16) / 8 + (g >> 1);
                *(u32x2*)(smem + row * 256 + ((c ^ r) * 16) + (g & 1) * 8) = o;
            }
    }
    }
    if constexpr (SILU) {
        asm volatile("s_waitcnt lgkmcnt(0)" ::: "memory");
        __builtin_amdgcn_s_barrier();
        const int c = lane & 15;
#pragma unroll
        for (int i = 0; i < BM / 32; ++i) {
            const int row = wid * (BM / 8) + i * 4 + (lane >> 4);
            typedef __attribute__((ext_vector_type(4))) unsigned u32x4;
            u32x4 v = *(const u32x4*)(smem + row * 256 + ((c ^ (row & 15)) * 16));
            u32x4 o;
#pragma unroll
            for (int e = 0; e < 4; ++e)
                o[e] = pack2(silu_f(__uint_as_float(v[e] << 16)), silu_f(__uint_as_float(v[e] & 0xffff0000u)));
            if (n0 + c * 8 < N) {
                u32x4* dst = (u32x4*)(C + (size_t)(m0 + row) * N + n0 + c * 8);
                *dst = o;
            }
        }
        if (wid < 4) {   }
    }
}

constexpr int G1_NS = 5;
__global__ __launch_bounds__(512, 2) void gemm1_kernel(const u16* __restrict__ x, const u16* __restrict__ w1, u16* __restrict__ h,
                                                       const u16* __restrict__ w2) {
    __shared__ __attribute__((aligned(128))) char smem[G1_NS * 32768];
    if (blockIdx.x >= 224) {
        const int w = __builtin_amdgcn_readfirstlane(threadIdx.x >> 6);
        const char* p = (const char*)w2 + (size_t)(blockIdx.x - 224) * (F_ * D_ * 2 / 32) + w * 8640;
        const unsigned dst = (unsigned)(uintptr_t)smem + w * 1024;
        const unsigned vo = (threadIdx.x & 63) * 16;
#pragma unroll 1
        for (int i = 0; i < 8; ++i) glds16(p + i * 1024, vo, dst);
        if ((threadIdx.x & 63) < 28) glds16(p + 8192, vo, dst);
        wait_vm<0>();
        return;
    }
    const int b = blockIdx.x, xcd = b & 7, idx = b >> 3;
    const int mb = (xcd >> 1) * 4 + (idx & 3), nb = (xcd & 1) * 7 + (idx >> 2);
    gemm_tile<128, 128, D_, F_, G1_NS, true>(x, w1, h, smem, mb * 128, nb * 128, ((idx >> 2) + 3 * (idx & 3)) % 10,
                                             (const char*)(x + (size_t)(xcd >> 1) * 512 * D_), 512 * 10, (const char*)(w1 + (xcd & 1) * 896), idx);
}

constexpr int G2_NS = 8;
__global__ __launch_bounds__(512, 2) void gemm2_kernel(const u16* __restrict__ h, const u16* __restrict__ w2, u16* __restrict__ out) {
    __shared__ __attribute__((aligned(128))) char smem[G2_NS * 20480];
    const int b = blockIdx.x, xcd = b & 7, idx = b >> 3;
    const int t = xcd * 28 + idx;
    if (t >= 220) return;
    const int mb = t / 10, nb = t - mb * 10;
    const int r0 = (xcd * 28) / 10 * 96, r1x = ((xcd * 28 + 27) / 10 + 1) * 96, r1 = r1x > M_ ? M_ : r1x;
    gemm_tile<96, 64, F_, D_, G2_NS, false>(h, w2, out, smem, mb * 96, nb * 64, (nb * 27 / 10 + (mb % 3) * 9) % 27,
                                            (const char*)(h + (size_t)r0 * F_), (r1 - r0) * 27, (const char*)w2, idx);
}

extern "C" void kernel_launch(void* const* d_in, const int* in_sizes, int n_in, void* d_out, int out_size, void* d_ws,
                              size_t ws_size, hipStream_t stream) {
    const u16* x = (const u16*)d_in[0];
    const u16* w1 = (const u16*)d_in[1];
    const u16* w2 = (const u16*)d_in[2];
    u16* h = (u16*)d_ws;
    u16* out = (u16*)d_out;
    gemm1_kernel<<<256, 512, 0, stream>>>(x, w1, h, w2);
    gemm2_kernel<<<224, 512, 0, stream>>>(h, w2, out);
}
```

```cpp
#include <hip/hip_runtime.h>
#include <hip/hip_bf16.h>
#include <stdint.h>
#include <type_traits>
#define ST_SC01

typedef unsigned short u16;
typedef __attribute__((ext_vector_type(8))) short bf16x8;
typedef __attribute__((ext_vector_type(4))) short s16x4;
typedef __attribute__((ext_vector_type(4))) float f32x4;
typedef __attribute__((ext_vector_type(2))) unsigned u32x2;
typedef __attribute__((address_space(3))) const char* lds_cptr;

constexpr int M_ = 2048, D_ = 640, F_ = 1728;

template <int I, int N, class Fn>
__device__ __forceinline__ void static_for(Fn&& f) {
    if constexpr (I < N) { f(std::integral_constant<int, I>{}); static_for<I + 1, N>(f); }
}
template <int N> __device__ __forceinline__ void wait_vm() { asm volatile("s_waitcnt vmcnt(%0)" ::"n"(N) : "memory"); }

__device__ __forceinline__ void glds16(const void* sbase, unsigned voff, unsigned lds_base) {
    unsigned keep;
    asm volatile("s_mov_b32 %0, m0\n\ts_mov_b32 m0, %3\n\ts_nop 0\n\tglobal_load_lds_dwordx4 %1, %2\n\ts_mov_b32 m0, %0"
                 : "=&s"(keep) : "v"(voff), "s"(sbase), "s"(lds_base) : "memory");
}
__device__ __forceinline__ bf16x8 lds_b128(lds_cptr p) { return *(const __attribute__((address_space(3))) bf16x8*)p; }
__device__ __forceinline__ s16x4 lds_tr(lds_cptr p) {
    return __builtin_amdgcn_ds_read_tr16_b64_v4i16((__attribute__((address_space(3))) s16x4*)p);
}
__device__ __forceinline__ void mfma16(f32x4& c, bf16x8 a, bf16x8 b) {
    asm volatile("v_mfma_f32_16x16x32_bf16 %0, %1, %2, %0" : "+a"(c) : "v"(a), "v"(b));
}
__device__ __forceinline__ float bf16_round(float v) { return (float)(__bf16)v; }
__device__ __forceinline__ unsigned pack2(float lo, float hi) {
    typedef __attribute__((ext_vector_type(2))) float f32x2;
    typedef __attribute__((ext_vector_type(2))) __bf16 bf16x2;
    f32x2 v = {lo, hi};
    bf16x2 r = __builtin_convertvector(v, bf16x2);
    return __builtin_bit_cast(unsigned, r);
}
__device__ __forceinline__ float silu_f(float v) { return v * __builtin_amdgcn_rcpf(1.0f + __expf(-v)); }

template <int BM, int BN, int K, int N, int NS, bool SILU>
__device__ __forceinline__ void gemm_tile(const u16* __restrict__ A, const u16* __restrict__ B, u16* __restrict__ C,
                                          char* smem, int m0, int n0, int kshift,
                                          const char* pfa, int pfa_lines, const char* pfb, int slice) {
    constexpr int NK = K / 64;
    constexpr int AB = BM * 128, BB = 64 * BN * 2, ST = AB + BB;
    constexpr int PA = BM / 32, PB = BB / 4096, NI = PA + PB;
    constexpr int WM = BM / 2, WN = BN / 2, TM = WM / 16, TN = WN / 16;
    const int tid = threadIdx.x, lane = tid & 63;
    const int wid = __builtin_amdgcn_readfirstlane(tid >> 6);
    asm volatile("" ::"s"(A), "s"(B), "s"(C));
    if (wid >= 4) {
        const int pw = wid - 4;
        unsigned voff[NI];
#pragma unroll
        for (int i = 0; i < PA; ++i) {
            int row = (pw * PA + i) * 8 + (lane >> 3);
            int ch = (lane & 7) ^ ((row >> 1) & 7);
            int gr = m0 + row; gr = gr > M_ - 1 ? M_ - 1 : gr;
            voff[i] = (unsigned)(gr * (K * 2) + ch * 16);
        }
#pragma unroll
        for (int i = 0; i < PB; ++i) {
            const int j = pw * PB + i;
            int k, cb;
            if constexpr (BN == 128) { k = j * 4 + (lane >> 4); cb = ((lane & 15) >> 1) ^ ((k & 3) | (((k >> 3) & 1) << 2)); }
            else { k = j * 8 + (lane >> 3); cb = ((lane & 7) >> 1) ^ (((k >> 1) & 1) | (((k >> 3) & 1) << 1)); }
            int col = n0 + cb * 16 + (lane & 1) * 8; col = col > N - 8 ? N - 8 : col;
            voff[PA + i] = (unsigned)(k * (N * 2) + col * 2);
        }
        const char* pa = (const char*)A + kshift * 128;
        const char* pb = (const char*)B + (size_t)kshift * (64 * N * 2);
        int kcur = kshift;
        const unsigned da = (unsigned)(uintptr_t)smem + pw * PA * 1024, db = (unsigned)(uintptr_t)smem + AB + pw * PB * 1024;
        unsigned slot = 0;
        auto issue = [&]() {
#pragma unroll
            for (int i = 0; i < PA; ++i) glds16(pa, voff[i], da + slot + i * 1024);
#pragma unroll
            for (int i = 0; i < PB; ++i) glds16(pb, voff[PA + i], db + slot + i * 1024);
            pa += 128; pb += 64 * N * 2;
            if (++kcur == NK) { kcur = 0; pa -= NK * 128; pb -= (size_t)NK * (64 * N * 2); }
            slot += ST; if (slot == NS * ST) slot = 0;
        };
        issue(); issue();
        wait_vm<NI>();
        __builtin_amdgcn_s_barrier();
#pragma unroll 1
        for (int i = 2; i < NS - 1; ++i) issue();
#pragma unroll 1
        for (int kt = 0; kt < NK - NS + 1; ++kt) {
            wait_vm<NI*(NS - 3)>();
            __builtin_amdgcn_s_barrier();
            issue();
        }
        static_for<0, NS - 2>([&](auto J) {
            wait_vm<NI*(NS - 3 - J.value)>();
            __builtin_amdgcn_s_barrier();
        });
        if constexpr (!SILU) return;
    } else {
    constexpr int PFLB = (BN == 128) ? 14 : 10, PFB_LINES = K * PFLB;
    constexpr int NPF = (BN == 128) ? 2 : 4;
    unsigned pfd[NPF];
    for (int i = 0; i < NPF; ++i) pfd[i] = 0;
    const int r = lane & 15, g = lane >> 4, q = (lane & 15) >> 2, p = lane & 3;
    const int wm = wid >> 1, wn = wid & 1;
    lds_cptr xa[2];
    {
        const int row = wm * WM + r;
#pragma unroll
        for (int s = 0; s < 2; ++s) xa[s] = (lds_cptr)smem + row * 128 + (((4 * s + g) ^ ((row >> 1) & 7)) * 16);
    }
    lds_cptr wa[2][TN];
#pragma unroll
    for (int t = 0; t < 2; ++t)
#pragma unroll
        for (int nt = 0; nt < TN; ++nt) {
            const int k = 8 * g + 4 * t + q, cb = wn * TN + nt;
            int sl;
            if constexpr (BN == 128) sl = cb ^ ((k & 3) | (((k >> 3) & 1) << 2));
            else sl = cb ^ (((k >> 1) & 1) | (((k >> 3) & 1) << 1));
            wa[t][nt] = (lds_cptr)smem + AB + k * (BN * 2) + sl * 32 + p * 8;
        }
    f32x4 acc[TM][TN];
#pragma unroll
    for (int mt = 0; mt < TM; ++mt)
#pragma unroll
        for (int nt = 0; nt < TN; ++nt) acc[mt][nt] = (f32x4){0.f, 0.f, 0.f, 0.f};

    bf16x8 af[2][TM];
    s16x4 bl[2][TN], bh[2][TN];
    constexpr int NR = TM + 2 * TN, NM = TM * TN;
    auto rd = [&](auto BUF, auto S, auto J, unsigned so) {
        constexpr int buf = BUF.value, s = S.value, j = J.value;
        if constexpr (j >= 2 && j < 2 + TM) af[buf][j - 2] = lds_b128(xa[s] + so + (j - 2) * 2048);
        else {
            constexpr int jb = j < 2 ? j : j - TM;
            if constexpr ((jb & 1) == 0) bl[buf][jb >> 1] = lds_tr(wa[0][jb >> 1] + so + s * 32 * BN * 2);
            else bh[buf][jb >> 1] = lds_tr(wa[1][jb >> 1] + so + s * 32 * BN * 2);
        }
    };
    auto phase = [&](auto BUF, auto SN, unsigned so, auto DORD) {
        constexpr int buf = BUF.value;
        static_for<0, NM>([&](auto J) {
            constexpr int j = J.value, nt = j / TM, mt = j % TM;
            mfma16(acc[mt][nt], __builtin_shufflevector(bl[buf][nt], bh[buf][nt], 0, 1, 2, 3, 4, 5, 6, 7), af[buf][mt]);
            if constexpr (DORD.value) {
                constexpr int NS_ = NM - NM / 4;
                constexpr int lo = (j < NS_ ? j : NS_) * NR / NS_, hi = (j + 1 < NS_ ? j + 1 : NS_) * NR / NS_;
                static_for<lo, hi>([&](auto R) { rd(std::integral_constant<int, buf ^ 1>{}, SN, R, so); });
            }
            __builtin_amdgcn_sched_barrier(0);
        });
    };
    using I0 = std::integral_constant<int, 0>;
    using I1 = std::integral_constant<int, 1>;
    __builtin_amdgcn_s_barrier();
    if constexpr (NPF == 2) asm volatile("s_waitcnt vmcnt(0)" ::"v"(pfd[0]), "v"(pfd[1]) : "memory");
    else asm volatile("s_waitcnt vmcnt(0)" ::"v"(pfd[0]), "v"(pfd[1]), "v"(pfd[2]), "v"(pfd[3]) : "memory");
    unsigned so = 0;
    static_for<0, NR>([&](auto R) { rd(I0{}, I0{}, R, 0u); });
#pragma unroll 1
    for (int kt = 0; kt < NK - 1; ++kt) {
        phase(I0{}, I1{}, so, I1{});
        __builtin_amdgcn_s_barrier();
        so += ST; if (so == NS * ST) so = 0;
        phase(I1{}, I0{}, so, I1{});
    }
    phase(I0{}, I1{}, so, I1{});
    phase(I1{}, I0{}, so, I0{});

    asm volatile("s_nop 15\n\ts_nop 15" ::: "memory");
    if constexpr (!SILU) {
#pragma unroll
        for (int mt = 0; mt < TM; ++mt) {
            const int row = m0 + wm * WM + mt * 16 + r;
            u16* crow = C + (size_t)row * N + n0 + wn * WN + 4 * g;
#pragma unroll
            for (int nt = 0; nt < TN; ++nt) {
                if (row < M_ && n0 + wn * WN + nt * 16 < N) {
                    f32x4 v = acc[mt][nt];
                    u32x2 o;
                    o.x = pack2(v[0], v[1]);
                    o.y = pack2(v[2], v[3]);
                    *(u32x2*)(crow + nt * 16) = o;
                }
            }
        }
        return;
    } else {
        static_assert(!SILU || ((NK - 1) % NS != 0 && (NK - 2) % NS != 0 && BM * BN * 2 <= ST && BN == 128), "epilogue slot");
#pragma unroll
        for (int mt = 0; mt < TM; ++mt)
#pragma unroll
            for (int nt = 0; nt < TN; ++nt) {
                f32x4 v = acc[mt][nt];
                u32x2 o;
                o.x = pack2(v[0], v[1]);
                o.y = pack2(v[2], v[3]);
                const int row = wm * WM + mt * 16 + r, c = (wn * WN + nt * 16) / 8 + (g >> 1);
                *(u32x2*)(smem + row * 256 + ((c ^ r) * 16) + (g & 1) * 8) = o;
            }
    }
    }
    if constexpr (SILU) {
        asm volatile("s_waitcnt lgkmcnt(0)" ::: "memory");
        __builtin_amdgcn_s_barrier();
        const int c = lane & 15;
#pragma unroll
        for (int i = 0; i < BM / 32; ++i) {
            const int row = wid * (BM / 8) + i * 4 + (lane >> 4);
            typedef __attribute__((ext_vector_type(4))) unsigned u32x4;
            u32x4 v = *(const u32x4*)(smem + row * 256 + ((c ^ (row & 15)) * 16));
            u32x4 o;
#pragma unroll
            for (int e = 0; e < 4; ++e)
                o[e] = pack2(silu_f(__uint_as_float(v[e] << 16)), silu_f(__uint_as_float(v[e] & 0xffff0000u)));
            if (n0 + c * 8 < N) {
                u32x4* dst = (u32x4*)(C + (size_t)(m0 + row) * N + n0 + c * 8);
                asm volatile("global_store_dwordx4 %0, %1, off sc0 sc1\n\ts_nop 1" ::"v"(dst), "v"(o) : "memory");
            }
        }
        if (wid < 4) {   }
    }
}

constexpr int G1_NS = 5;
__global__ __launch_bounds__(512, 2) void gemm1_kernel(const u16* __restrict__ x, const u16* __restrict__ w1, u16* __restrict__ h,
                                                       const u16* __restrict__ w2) {
    __shared__ __attribute__((aligned(128))) char smem[G1_NS * 32768];
    if (blockIdx.x >= 224) {
        const int w = __builtin_amdgcn_readfirstlane(threadIdx.x >> 6);
        const char* p = (const char*)w2 + (size_t)(blockIdx.x - 224) * (F_ * D_ * 2 / 32) + w * 8640;
        const unsigned dst = (unsigned)(uintptr_t)smem + w * 1024;
        const unsigned vo = (threadIdx.x & 63) * 16;
#pragma unroll 1
        for (int i = 0; i < 8; ++i) glds16(p + i * 1024, vo, dst);
        if ((threadIdx.x & 63) < 28) glds16(p + 8192, vo, dst);
        wait_vm<0>();
        return;
    }
    const int b = blockIdx.x, xcd = b & 7, idx = b >> 3;
    const int mb = (xcd >> 1) * 4 + (idx & 3), nb = (xcd & 1) * 7 + (idx >> 2);
    gemm_tile<128, 128, D_, F_, G1_NS, true>(x, w1, h, smem, mb * 128, nb * 128,
                                             ((idx >> 2) + 3 * (idx & 3)) % 10,

                                             (const char*)(x + (size_t)(xcd >> 1) * 512 * D_), 512 * 10, (const char*)(w1 + (xcd & 1) * 896), idx);
}

constexpr int G2_NS = 8;
__global__ __launch_bounds__(512, 2) void gemm2_kernel(const u16* __restrict__ h, const u16* __restrict__ w2, u16* __restrict__ out) {
    __shared__ __attribute__((aligned(128))) char smem[G2_NS * 20480];
    const int b = blockIdx.x, xcd = b & 7, idx = b >> 3;
    const int t = xcd * 28 + idx;
    if (t >= 220) return;
    const int mb = t / 10, nb = t - mb * 10;
    const int r0 = (xcd * 28) / 10 * 96, r1x = ((xcd * 28 + 27) / 10 + 1) * 96, r1 = r1x > M_ ? M_ : r1x;
    gemm_tile<96, 64, F_, D_, G2_NS, false>(h, w2, out, smem, mb * 96, nb * 64,
                                            (nb * 27 / 10 + (mb % 3) * 9) % 27,

                                            (const char*)(h + (size_t)r0 * F_), (r1 - r0) * 27, (const char*)w2, idx);
}

extern "C" void kernel_launch(void* const* d_in, const int* in_sizes, int n_in, void* d_out, int out_size, void* d_ws,
                              size_t ws_size, hipStream_t stream) {
    const u16* x = (const u16*)d_in[0];
    const u16* w1 = (const u16*)d_in[1];
    const u16* w2 = (const u16*)d_in[2];
    u16* h = (u16*)d_ws;
    u16* out = (u16*)d_out;
    gemm1_kernel<<<256, 512, 0, stream>>>(x, w1, h, w2);
    gemm2_kernel<<<224, 512, 0, stream>>>(h, w2, out);
}
```

```cpp
#include <hip/hip_runtime.h>
#include <hip/hip_bf16.h>
#include <stdint.h>
#include <type_traits>
#define ST_SC01
#define ROT0

typedef unsigned short u16;
typedef __attribute__((ext_vector_type(8))) short bf16x8;
typedef __attribute__((ext_vector_type(4))) short s16x4;
typedef __attribute__((ext_vector_type(4))) float f32x4;
typedef __attribute__((ext_vector_type(2))) unsigned u32x2;
typedef __attribute__((address_space(3))) const char* lds_cptr;

constexpr int M_ = 2048, D_ = 640, F_ = 1728;

template <int I, int N, class Fn>
__device__ __forceinline__ void static_for(Fn&& f) {
    if constexpr (I < N) { f(std::integral_constant<int, I>{}); static_for<I + 1, N>(f); }
}
template <int N> __device__ __forceinline__ void wait_vm() { asm volatile("s_waitcnt vmcnt(%0)" ::"n"(N) : "memory"); }

__device__ __forceinline__ void glds16(const void* sbase, unsigned voff, unsigned lds_base) {
    unsigned keep;
    asm volatile("s_mov_b32 %0, m0\n\ts_mov_b32 m0, %3\n\ts_nop 0\n\tglobal_load_lds_dwordx4 %1, %2\n\ts_mov_b32 m0, %0"
                 : "=&s"(keep) : "v"(voff), "s"(sbase), "s"(lds_base) : "memory");
}
__device__ __forceinline__ bf16x8 lds_b128(lds_cptr p) { return *(const __attribute__((address_space(3))) bf16x8*)p; }
__device__ __forceinline__ s16x4 lds_tr(lds_cptr p) {
    return __builtin_amdgcn_ds_read_tr16_b64_v4i16((__attribute__((address_space(3))) s16x4*)p);
}
__device__ __forceinline__ void mfma16(f32x4& c, bf16x8 a, bf16x8 b) {
    asm volatile("v_mfma_f32_16x16x32_bf16 %0, %1, %2, %0" : "+a"(c) : "v"(a), "v"(b));
}
__device__ __forceinline__ float bf16_round(float v) { return (float)(__bf16)v; }
__device__ __forceinline__ unsigned pack2(float lo, float hi) {
    typedef __attribute__((ext_vector_type(2))) float f32x2;
    typedef __attribute__((ext_vector_type(2))) __bf16 bf16x2;
    f32x2 v = {lo, hi};
    bf16x2 r = __builtin_convertvector(v, bf16x2);
    return __builtin_bit_cast(unsigned, r);
}
__device__ __forceinline__ float silu_f(float v) { return v * __builtin_amdgcn_rcpf(1.0f + __expf(-v)); }

template <int BM, int BN, int K, int N, int NS, bool SILU>
__device__ __forceinline__ void gemm_tile(const u16* __restrict__ A, const u16* __restrict__ B, u16* __restrict__ C,
                                          char* smem, int m0, int n0, int kshift,
                                          const char* pfa, int pfa_lines, const char* pfb, int slice) {
    constexpr int NK = K / 64;
    constexpr int AB = BM * 128, BB = 64 * BN * 2, ST = AB + BB;
    constexpr int PA = BM / 32, PB = BB / 4096, NI = PA + PB;
    constexpr int WM = BM / 2, WN = BN / 2, TM = WM / 16, TN = WN / 16;
    const int tid = threadIdx.x, lane = tid & 63;
    const int wid = __builtin_amdgcn_readfirstlane(tid >> 6);
    asm volatile("" ::"s"(A), "s"(B), "s"(C));
    if (wid >= 4) {
        const int pw = wid - 4;
        unsigned voff[NI];
#pragma unroll
        for (int i = 0; i < PA; ++i) {
            int row = (pw * PA + i) * 8 + (lane >> 3);
            int ch = (lane & 7) ^ ((row >> 1) & 7);
            if constexpr (SILU) {
                voff[i] = (unsigned)((m0 + row) * (K * 2) + ch * 16);
            } else {
                int rb = (m0 >> 3) + pw * PA + i; rb = rb > M_ / 8 - 1 ? M_ / 8 - 1 : rb;
                voff[i] = (unsigned)(rb * (NK * 1024) + (lane >> 3) * 128 + ch * 16);
            }
        }
#pragma unroll
        for (int i = 0; i < PB; ++i) {
            const int j = pw * PB + i;
            int k, cb;
            if constexpr (BN == 128) { k = j * 4 + (lane >> 4); cb = ((lane & 15) >> 1) ^ ((k & 3) | (((k >> 3) & 1) << 2)); }
            else { k = j * 8 + (lane >> 3); cb = ((lane & 7) >> 1) ^ (((k >> 1) & 1) | (((k >> 3) & 1) << 1)); }
            int col = n0 + cb * 16 + (lane & 1) * 8; col = col > N - 8 ? N - 8 : col;
            voff[PA + i] = (unsigned)(k * (N * 2) + col * 2);
        }
        constexpr int ASTEP = SILU ? 128 : 1024;
        const char* pa = (const char*)A + kshift * ASTEP;
        const char* pb = (const char*)B + (size_t)kshift * (64 * N * 2);
        int kcur = kshift;
        const unsigned da = (unsigned)(uintptr_t)smem + pw * PA * 1024, db = (unsigned)(uintptr_t)smem + AB + pw * PB * 1024;
        unsigned slot = 0;
        auto issue = [&]() {
#pragma unroll
            for (int i = 0; i < PA; ++i) glds16(pa, voff[i], da + slot + i * 1024);
#pragma unroll
            for (int i = 0; i < PB; ++i) glds16(pb, voff[PA + i], db + slot + i * 1024);
            pa += ASTEP; pb += 64 * N * 2;
            if (++kcur == NK) { kcur = 0; pa -= NK * ASTEP; pb -= (size_t)NK * (64 * N * 2); }
            slot += ST; if (slot == NS * ST) slot = 0;
        };
        issue(); issue();
        wait_vm<NI>();
        __builtin_amdgcn_s_barrier();
#pragma unroll 1
        for (int i = 2; i < NS - 1; ++i) issue();
#pragma unroll 1
        for (int kt = 0; kt < NK - NS + 1; ++kt) {
            wait_vm<NI*(NS - 3)>();
            __builtin_amdgcn_s_barrier();
            issue();
        }
        static_for<0, NS - 2>([&](auto J) {
            wait_vm<NI*(NS - 3 - J.value)>();
            __builtin_amdgcn_s_barrier();
        });
        if constexpr (!SILU) return;
    } else {
    constexpr int PFLB = (BN == 128) ? 14 : 10, PFB_LINES = K * PFLB;
    constexpr int NPF = (BN == 128) ? 2 : 4;
    unsigned pfd[NPF];
    for (int i = 0; i < NPF; ++i) pfd[i] = 0;
    const int r = lane & 15, g = lane >> 4, q = (lane & 15) >> 2, p = lane & 3;
    const int wm = wid >> 1, wn = wid & 1;
    lds_cptr xa[2];
    {
        const int row = wm * WM + r;
#pragma unroll
        for (int s = 0; s < 2; ++s) xa[s] = (lds_cptr)smem + row * 128 + (((4 * s + g) ^ ((row >> 1) & 7)) * 16);
    }
    lds_cptr wa[2][TN];
#pragma unroll
    for (int t = 0; t < 2; ++t)
#pragma unroll
        for (int nt = 0; nt < TN; ++nt) {
            const int k = 8 * g + 4 * t + q, cb = wn * TN + nt;
            int sl;
            if constexpr (BN == 128) sl = cb ^ ((k & 3) | (((k >> 3) & 1) << 2));
            else sl = cb ^ (((k >> 1) & 1) | (((k >> 3) & 1) << 1));
            wa[t][nt] = (lds_cptr)smem + AB + k * (BN * 2) + sl * 32 + p * 8;
        }
    f32x4 acc[TM][TN];
#pragma unroll
    for (int mt = 0; mt < TM; ++mt)
#pragma unroll
        for (int nt = 0; nt < TN; ++nt) acc[mt][nt] = (f32x4){0.f, 0.f, 0.f, 0.f};

    bf16x8 af[2][TM];
    s16x4 bl[2][TN], bh[2][TN];
    constexpr int NR = TM + 2 * TN, NM = TM * TN;
    auto rd = [&](auto BUF, auto S, auto J, unsigned so) {
        constexpr int buf = BUF.value, s = S.value, j = J.value;
        if constexpr (j >= 2 && j < 2 + TM) af[buf][j - 2] = lds_b128(xa[s] + so + (j - 2) * 2048);
        else {
            constexpr int jb = j < 2 ? j : j - TM;
            if constexpr ((jb & 1) == 0) bl[buf][jb >> 1] = lds_tr(wa[0][jb >> 1] + so + s * 32 * BN * 2);
            else bh[buf][jb >> 1] = lds_tr(wa[1][jb >> 1] + so + s * 32 * BN * 2);
        }
    };
    auto phase = [&](auto BUF, auto SN, unsigned so, auto DORD) {
        constexpr int buf = BUF.value;
        static_for<0, NM>([&](auto J) {
            constexpr int j = J.value, nt = j / TM, mt = j % TM;
            mfma16(acc[mt][nt], __builtin_shufflevector(bl[buf][nt], bh[buf][nt], 0, 1, 2, 3, 4, 5, 6, 7), af[buf][mt]);
            if constexpr (DORD.value) {
                constexpr int NS_ = NM - NM / 4;
                constexpr int lo = (j < NS_ ? j : NS_) * NR / NS_, hi = (j + 1 < NS_ ? j + 1 : NS_) * NR / NS_;
                static_for<lo, hi>([&](auto R) { rd(std::integral_constant<int, buf ^ 1>{}, SN, R, so); });
            }
            __builtin_amdgcn_sched_barrier(0);
        });
    };
    using I0 = std::integral_constant<int, 0>;
    using I1 = std::integral_constant<int, 1>;
    __builtin_amdgcn_s_barrier();
    if constexpr (NPF == 2) asm volatile("s_waitcnt vmcnt(0)" ::"v"(pfd[0]), "v"(pfd[1]) : "memory");
    else asm volatile("s_waitcnt vmcnt(0)" ::"v"(pfd[0]), "v"(pfd[1]), "v"(pfd[2]), "v"(pfd[3]) : "memory");
    unsigned so = 0;
    static_for<0, NR>([&](auto R) { rd(I0{}, I0{}, R, 0u); });
#pragma unroll 1
    for (int kt = 0; kt < NK - 1; ++kt) {
        phase(I0{}, I1{}, so, I1{});
        __builtin_amdgcn_s_barrier();
        so += ST; if (so == NS * ST) so = 0;
        phase(I1{}, I0{}, so, I1{});
    }
    phase(I0{}, I1{}, so, I1{});
    phase(I1{}, I0{}, so, I0{});

    asm volatile("s_nop 15\n\ts_nop 15" ::: "memory");
    if constexpr (!SILU) {
#pragma unroll
        for (int mt = 0; mt < TM; ++mt) {
            const int row = m0 + wm * WM + mt * 16 + r;
            u16* crow = C + (size_t)row * N + n0 + wn * WN + 4 * g;
#pragma unroll
            for (int nt = 0; nt < TN; ++nt) {
                if (row < M_ && n0 + wn * WN + nt * 16 < N) {
                    f32x4 v = acc[mt][nt];
                    u32x2 o;
                    o.x = pack2(v[0], v[1]);
                    o.y = pack2(v[2], v[3]);
                    *(u32x2*)(crow + nt * 16) = o;
                }
            }
        }
        return;
    } else {
        static_assert(!SILU || ((NK - 1) % NS != 0 && (NK - 2) % NS != 0 && BM * BN * 2 <= ST && BN == 128), "epilogue slot");
#pragma unroll
        for (int mt = 0; mt < TM; ++mt)
#pragma unroll
            for (int nt = 0; nt < TN; ++nt) {
                f32x4 v = acc[mt][nt];
                u32x2 o;
                o.x = pack2(v[0], v[1]);
                o.y = pack2(v[2], v[3]);
                const int row = wm * WM + mt * 16 + r, c = (wn * WN + nt * 16) / 8 + (g >> 1);
                *(u32x2*)(smem + row * 256 + ((c ^ r) * 16) + (g & 1) * 8) = o;
            }
    }
    }
    if constexpr (SILU) {
        asm volatile("s_waitcnt lgkmcnt(0)" ::: "memory");
        __builtin_amdgcn_s_barrier();
        const int c = lane & 15;
#pragma unroll
        for (int i = 0; i < BM / 32; ++i) {
            const int row = wid * (BM / 8) + i * 4 + (lane >> 4);
            typedef __attribute__((ext_vector_type(4))) unsigned u32x4;
            u32x4 v = *(const u32x4*)(smem + row * 256 + ((c ^ (row & 15)) * 16));
            u32x4 o;
#pragma unroll
            for (int e = 0; e < 4; ++e)
                o[e] = pack2(silu_f(__uint_as_float(v[e] << 16)), silu_f(__uint_as_float(v[e] & 0xffff0000u)));
            if (n0 + c * 8 < N) {
                u32x4* dst = (u32x4*)((char*)C + (size_t)(((m0 + row) >> 3) * (N / 64) + (n0 >> 6) + (c >> 3)) * 1024 + (row & 7) * 128 + (c & 7) * 16);
                asm volatile("global_store_dwordx4 %0, %1, off sc0 sc1\n\ts_nop 1" ::"v"(dst), "v"(o) : "memory");
            }
        }
        if (wid < 4) {   }
    }
}

constexpr int G1_NS = 5;
__global__ __launch_bounds__(512, 2) void gemm1_kernel(const u16* __restrict__ x, const u16* __restrict__ w1, u16* __restrict__ h,
                                                       const u16* __restrict__ w2) {
    __shared__ __attribute__((aligned(128))) char smem[G1_NS * 32768];
    if (blockIdx.x >= 224) {
        const int w = __builtin_amdgcn_readfirstlane(threadIdx.x >> 6);
        const char* p = (const char*)w2 + (size_t)(blockIdx.x - 224) * (F_ * D_ * 2 / 32) + w * 8640;
        const unsigned dst = (unsigned)(uintptr_t)smem + w * 1024;
        const unsigned vo = (threadIdx.x & 63) * 16;
#pragma unroll 1
        for (int i = 0; i < 8; ++i) glds16(p + i * 1024, vo, dst);
        if ((threadIdx.x & 63) < 28) glds16(p + 8192, vo, dst);
        wait_vm<0>();
        return;
    }
    const int b = blockIdx.x, xcd = b & 7, idx = b >> 3;
    const int mb = (xcd >> 1) * 4 + (idx & 3), nb = (xcd & 1) * 7 + (idx >> 2);
    gemm_tile<128, 128, D_, F_, G1_NS, true>(x, w1, h, smem, mb * 128, nb * 128,
                                             0,

                                             (const char*)(x + (size_t)(xcd >> 1) * 512 * D_), 512 * 10, (const char*)(w1 + (xcd & 1) * 896), idx);
}

constexpr int G2_NS = 8;
__global__ __launch_bounds__(512, 2) void gemm2_kernel(const u16* __restrict__ h, const u16* __restrict__ w2, u16* __restrict__ out) {
    __shared__ __attribute__((aligned(128))) char smem[G2_NS * 20480];
    const int b = blockIdx.x, xcd = b & 7, idx = b >> 3;
    const int t = xcd * 28 + idx;
    if (t >= 220) return;
    const int mb = t / 10, nb = t - mb * 10;
    const int r0 = (xcd * 28) / 10 * 96, r1x = ((xcd * 28 + 27) / 10 + 1) * 96, r1 = r1x > M_ ? M_ : r1x;
    gemm_tile<96, 64, F_, D_, G2_NS, false>(h, w2, out, smem, mb * 96, nb * 64,
                                            0,

                                            (const char*)(h + (size_t)r0 * F_), (r1 - r0) * 27, (const char*)w2, idx);
}

extern "C" void kernel_launch(void* const* d_in, const int* in_sizes, int n_in, void* d_out, int out_size, void* d_ws,
                              size_t ws_size, hipStream_t stream) {
    const u16* x = (const u16*)d_in[0];
    const u16* w1 = (const u16*)d_in[1];
    const u16* w2 = (const u16*)d_in[2];
    u16* h = (u16*)d_ws;
    u16* out = (u16*)d_out;
    gemm1_kernel<<<256, 512, 0, stream>>>(x, w1, h, w2);
    gemm2_kernel<<<224, 512, 0, stream>>>(h, w2, out);
}
```

```cpp
#include <hip/hip_runtime.h>
#include <hip/hip_bf16.h>
#include <stdint.h>
#include <type_traits>
#define ST_SC01
#define ROT0

typedef unsigned short u16;
typedef __attribute__((ext_vector_type(8))) short bf16x8;
typedef __attribute__((ext_vector_type(4))) short s16x4;
typedef __attribute__((ext_vector_type(4))) float f32x4;
typedef __attribute__((ext_vector_type(2))) unsigned u32x2;
typedef __attribute__((address_space(3))) const char* lds_cptr;

constexpr int M_ = 2048, D_ = 640, F_ = 1728;

template <int I, int N, class Fn>
__device__ __forceinline__ void static_for(Fn&& f) {
    if constexpr (I < N) { f(std::integral_constant<int, I>{}); static_for<I + 1, N>(f); }
}
template <int N> __device__ __forceinline__ void wait_vm() { asm volatile("s_waitcnt vmcnt(%0)" ::"n"(N) : "memory"); }

__device__ __forceinline__ void glds16(const void* sbase, unsigned voff, unsigned lds_base) {
    unsigned keep;
    asm volatile("s_mov_b32 %0, m0\n\ts_mov_b32 m0, %3\n\ts_nop 0\n\tglobal_load_lds_dwordx4 %1, %2\n\ts_mov_b32 m0, %0"
                 : "=&s"(keep) : "v"(voff), "s"(sbase), "s"(lds_base) : "memory");
}
__device__ __forceinline__ bf16x8 lds_b128(lds_cptr p) { return *(const __attribute__((address_space(3))) bf16x8*)p; }
__device__ __forceinline__ s16x4 lds_tr(lds_cptr p) {
    return __builtin_amdgcn_ds_read_tr16_b64_v4i16((__attribute__((address_space(3))) s16x4*)p);
}
__device__ __forceinline__ void mfma16(f32x4& c, bf16x8 a, bf16x8 b) {
    asm volatile("v_mfma_f32_16x16x32_bf16 %0, %1, %2, %0" : "+a"(c) : "v"(a), "v"(b));
}
__device__ __forceinline__ float bf16_round(float v) { return (float)(__bf16)v; }
__device__ __forceinline__ unsigned pack2(float lo, float hi) {
    typedef __attribute__((ext_vector_type(2))) float f32x2;
    typedef __attribute__((ext_vector_type(2))) __bf16 bf16x2;
    f32x2 v = {lo, hi};
    bf16x2 r = __builtin_convertvector(v, bf16x2);
    return __builtin_bit_cast(unsigned, r);
}
__device__ __forceinline__ float silu_f(float v) { return v * __builtin_amdgcn_rcpf(1.0f + __expf(-v)); }

template <int BM, int BN, int K, int N, int NS, bool SILU>
__device__ __forceinline__ void gemm_tile(const u16* __restrict__ A, const u16* __restrict__ B, u16* __restrict__ C,
                                          char* smem, int m0, int n0, int kshift,
                                          const char* pfa, int pfa_lines, const char* pfb, int slice) {
    constexpr int NK = K / 64;
    constexpr int AB = BM * 128, BB = 64 * BN * 2, ST = AB + BB;
    constexpr int PA = BM / 32, PB = BB / 4096, NI = PA + PB;
    constexpr int WM = BM / 2, WN = BN / 2, TM = WM / 16, TN = WN / 16;
    const int tid = threadIdx.x, lane = tid & 63;
    const int wid = __builtin_amdgcn_readfirstlane(tid >> 6);
    asm volatile("" ::"s"(A), "s"(B), "s"(C));
    if (wid >= 4) {
        const int pw = wid - 4;
        unsigned voff[NI];
#pragma unroll
        for (int i = 0; i < PA; ++i) {
            int row = (pw * PA + i) * 8 + (lane >> 3);
            int ch = (lane & 7) ^ ((row >> 1) & 7);
            if constexpr (SILU) {
                voff[i] = (unsigned)((m0 + row) * (K * 2) + ch * 16);
            } else {
                int rb = (m0 >> 3) + pw * PA + i; rb = rb > M_ / 8 - 1 ? M_ / 8 - 1 : rb;
                voff[i] = (unsigned)(rb * (NK * 1024) + (lane >> 3) * 128 + ch * 16);
            }
        }
#pragma unroll
        for (int i = 0; i < PB; ++i) {
            const int j = pw * PB + i;
            int k, cb;
            if constexpr (BN == 128) { k = j * 4 + (lane >> 4); cb = ((lane & 15) >> 1) ^ ((k & 3) | (((k >> 3) & 1) << 2)); }
            else { k = j * 8 + (lane >> 3); cb = ((lane & 7) >> 1) ^ (((k >> 1) & 1) | (((k >> 3) & 1) << 1)); }
            int col = n0 + cb * 16 + (lane & 1) * 8; col = col > N - 8 ? N - 8 : col;
            voff[PA + i] = (unsigned)(k * (N * 2) + col * 2);
        }
        constexpr int ASTEP = SILU ? 128 : 1024;
        const char* pa = (const char*)A + kshift * ASTEP;
        const char* pb = (const char*)B + (size_t)kshift * (64 * N * 2);
        int kcur = kshift;
        const unsigned da = (unsigned)(uintptr_t)smem + pw * PA * 1024, db = (unsigned)(uintptr_t)smem + AB + pw * PB * 1024;
        unsigned slot = 0;
        auto issue = [&]() {
#pragma unroll
            for (int i = 0; i < PA; ++i) glds16(pa, voff[i], da + slot + i * 1024);
#pragma unroll
            for (int i = 0; i < PB; ++i) glds16(pb, voff[PA + i], db + slot + i * 1024);
            pa += ASTEP; pb += 64 * N * 2;
            if (++kcur == NK) { kcur = 0; pa -= NK * ASTEP; pb -= (size_t)NK * (64 * N * 2); }
            slot += ST; if (slot == NS * ST) slot = 0;
        };
#define PRE 2
#pragma unroll 1
        for (int i = 0; i < PRE; ++i) issue();
        wait_vm<NI*(PRE - 1)>();
        __builtin_amdgcn_s_barrier();
#pragma unroll 1
        for (int i = PRE; i < NS - 1; ++i) issue();
#pragma unroll 1
        for (int kt = 0; kt < NK - NS + 1; ++kt) {
            wait_vm<NI*(NS - 3)>();
            __builtin_amdgcn_s_barrier();
            issue();
        }
        static_for<0, NS - 2>([&](auto J) {
            wait_vm<NI*(NS - 3 - J.value)>();
            __builtin_amdgcn_s_barrier();
        });
        if constexpr (!SILU) return;
    } else {
    constexpr int PFLB = (BN == 128) ? 14 : 10, PFB_LINES = K * PFLB;
    constexpr int NPF = (BN == 128) ? 2 : 4;
    unsigned pfd[NPF];
    for (int i = 0; i < NPF; ++i) pfd[i] = 0;
    const int r = lane & 15, g = lane >> 4, q = (lane & 15) >> 2, p = lane & 3;
    const int wm = wid >> 1, wn = wid & 1;
    lds_cptr xa[2];
    {
        const int row = wm * WM + r;
#pragma unroll
        for (int s = 0; s < 2; ++s) xa[s] = (lds_cptr)smem + row * 128 + (((4 * s + g) ^ ((row >> 1) & 7)) * 16);
    }
    lds_cptr wa[2][TN];
#pragma unroll
    for (int t = 0; t < 2; ++t)
#pragma unroll
        for (int nt = 0; nt < TN; ++nt) {
            const int k = 8 * g + 4 * t + q, cb = wn * TN + nt;
            int sl;
            if constexpr (BN == 128) sl = cb ^ ((k & 3) | (((k >> 3) & 1) << 2));
            else sl = cb ^ (((k >> 1) & 1) | (((k >> 3) & 1) << 1));
            wa[t][nt] = (lds_cptr)smem + AB + k * (BN * 2) + sl * 32 + p * 8;
        }
    f32x4 acc[TM][TN];
#pragma unroll
    for (int mt = 0; mt < TM; ++mt)
#pragma unroll
        for (int nt = 0; nt < TN; ++nt) acc[mt][nt] = (f32x4){0.f, 0.f, 0.f, 0.f};

    bf16x8 af[2][TM];
    s16x4 bl[2][TN], bh[2][TN];
    constexpr int NR = TM + 2 * TN, NM = TM * TN;
    auto rd = [&](auto BUF, auto S, auto J, unsigned so) {
        constexpr int buf = BUF.value, s = S.value, j = J.value;
        if constexpr (j >= 2 && j < 2 + TM) af[buf][j - 2] = lds_b128(xa[s] + so + (j - 2) * 2048);
        else {
            constexpr int jb = j < 2 ? j : j - TM;
            if constexpr ((jb & 1) == 0) bl[buf][jb >> 1] = lds_tr(wa[0][jb >> 1] + so + s * 32 * BN * 2);
            else bh[buf][jb >> 1] = lds_tr(wa[1][jb >> 1] + so + s * 32 * BN * 2);
        }
    };
    auto phase = [&](auto BUF, auto SN, unsigned so, auto DORD) {
        constexpr int buf = BUF.value;
        static_for<0, NM>([&](auto J) {
            constexpr int j = J.value, nt = j / TM, mt = j % TM;
            mfma16(acc[mt][nt], __builtin_shufflevector(bl[buf][nt], bh[buf][nt], 0, 1, 2, 3, 4, 5, 6, 7), af[buf][mt]);
            if constexpr (DORD.value) {
                constexpr int NS_ = NM - NM / 4;
                constexpr int lo = (j < NS_ ? j : NS_) * NR / NS_, hi = (j + 1 < NS_ ? j + 1 : NS_) * NR / NS_;
                static_for<lo, hi>([&](auto R) { rd(std::integral_constant<int, buf ^ 1>{}, SN, R, so); });
            }
            __builtin_amdgcn_sched_barrier(0);
        });
    };
    using I0 = std::integral_constant<int, 0>;
    using I1 = std::integral_constant<int, 1>;
    __builtin_amdgcn_s_barrier();
    if constexpr (NPF == 2) asm volatile("s_waitcnt vmcnt(0)" ::"v"(pfd[0]), "v"(pfd[1]) : "memory");
    else asm volatile("s_waitcnt vmcnt(0)" ::"v"(pfd[0]), "v"(pfd[1]), "v"(pfd[2]), "v"(pfd[3]) : "memory");
    unsigned so = 0;
    static_for<0, NR>([&](auto R) { rd(I0{}, I0{}, R, 0u); });
#pragma unroll 1
    for (int kt = 0; kt < NK - 1; ++kt) {
        phase(I0{}, I1{}, so, I1{});
        __builtin_amdgcn_s_barrier();
        so += ST; if (so == NS * ST) so = 0;
        phase(I1{}, I0{}, so, I1{});
    }
    phase(I0{}, I1{}, so, I1{});
    phase(I1{}, I0{}, so, I0{});

    asm volatile("s_nop 15\n\ts_nop 15" ::: "memory");
    if constexpr (!SILU) {
#pragma unroll
        for (int mt = 0; mt < TM; ++mt) {
            const int row = m0 + wm * WM + mt * 16 + r;
            u16* crow = C + (size_t)row * N + n0 + wn * WN + 4 * g;
#pragma unroll
            for (int nt = 0; nt < TN; ++nt) {
                if (row < M_ && n0 + wn * WN + nt * 16 < N) {
                    f32x4 v = acc[mt][nt];
                    u32x2 o;
                    o.x = pack2(v[0], v[1]);
                    o.y = pack2(v[2], v[3]);
                    *(u32x2*)(crow + nt * 16) = o;
                }
            }
        }
        return;
    } else {
        static_assert(!SILU || ((NK - 1) % NS != 0 && (NK - 2) % NS != 0 && BM * BN * 2 <= ST && BN == 128), "epilogue slot");
#pragma unroll
        for (int mt = 0; mt < TM; ++mt)
#pragma unroll
            for (int nt = 0; nt < TN; ++nt) {
                f32x4 v = acc[mt][nt];
                u32x2 o;
                o.x = pack2(v[0], v[1]);
                o.y = pack2(v[2], v[3]);
                const int row = wm * WM + mt * 16 + r, c = (wn * WN + nt * 16) / 8 + (g >> 1);
                *(u32x2*)(smem + row * 256 + ((c ^ r) * 16) + (g & 1) * 8) = o;
            }
    }
    }
    if constexpr (SILU) {
        asm volatile("s_waitcnt lgkmcnt(0)" ::: "memory");
        __builtin_amdgcn_s_barrier();
        const int c = lane & 15;
#pragma unroll
        for (int i = 0; i < BM / 32; ++i) {
            const int row = wid * (BM / 8) + i * 4 + (lane >> 4);
            typedef __attribute__((ext_vector_type(4))) unsigned u32x4;
            u32x4 v = *(const u32x4*)(smem + row * 256 + ((c ^ (row & 15)) * 16));
            u32x4 o;
#pragma unroll
            for (int e = 0; e < 4; ++e)
                o[e] = pack2(silu_f(__uint_as_float(v[e] << 16)), silu_f(__uint_as_float(v[e] & 0xffff0000u)));
            if (n0 + c * 8 < N) {
                u32x4* dst = (u32x4*)((char*)C + (size_t)(((m0 + row) >> 3) * (N / 64) + (n0 >> 6) + (c >> 3)) * 1024 + (row & 7) * 128 + (c & 7) * 16);
                asm volatile("global_store_dwordx4 %0, %1, off sc0 sc1\n\ts_nop 1" ::"v"(dst), "v"(o) : "memory");
            }
        }
        if (wid < 4) {   }
    }
}

constexpr int G1_NS = 5;
__global__ __launch_bounds__(512, 2) void gemm1_kernel(const u16* __restrict__ x, const u16* __restrict__ w1, u16* __restrict__ h,
                                                       const u16* __restrict__ w2) {
    __shared__ __attribute__((aligned(128))) char smem[G1_NS * 32768];
    if (blockIdx.x >= 224) {
        const int w = __builtin_amdgcn_readfirstlane(threadIdx.x >> 6);
        const char* p = (const char*)w2 + (size_t)(blockIdx.x - 224) * (F_ * D_ * 2 / 32) + w * 8640;
        const unsigned dst = (unsigned)(uintptr_t)smem + w * 1024;
        const unsigned vo = (threadIdx.x & 63) * 16;
#pragma unroll 1
        for (int i = 0; i < 8; ++i) glds16(p + i * 1024, vo, dst);
        if ((threadIdx.x & 63) < 28) glds16(p + 8192, vo, dst);
        wait_vm<0>();
        return;
    }
    const int b = blockIdx.x, xcd = b & 7, idx = b >> 3;
    const int mb = (xcd >> 1) * 4 + (idx & 3), nb = (xcd & 1) * 7 + (idx >> 2);
    gemm_tile<128, 128, D_, F_, G1_NS, true>(x, w1, h, smem, mb * 128, nb * 128,
                                             0,

                                             (const char*)(x + (size_t)(xcd >> 1) * 512 * D_), 512 * 10, (const char*)(w1 + (xcd & 1) * 896), idx);
}

constexpr int G2_NS = 8;
__global__ __launch_bounds__(512, 2) void gemm2_kernel(const u16* __restrict__ h, const u16* __restrict__ w2, u16* __restrict__ out) {
    __shared__ __attribute__((aligned(128))) char smem[G2_NS * 20480];
    const int b = blockIdx.x, xcd = b & 7, idx = b >> 3;
    const int t = xcd * 28 + idx;
    if (t >= 220) return;
    const int mb = t / 10, nb = t - mb * 10;
    const int r0 = (xcd * 28) / 10 * 96, r1x = ((xcd * 28 + 27) / 10 + 1) * 96, r1 = r1x > M_ ? M_ : r1x;
    gemm_tile<96, 64, F_, D_, G2_NS, false>(h, w2, out, smem, mb * 96, nb * 64,
                                            0,

                                            (const char*)(h + (size_t)r0 * F_), (r1 - r0) * 27, (const char*)w2, idx);
}

extern "C" void kernel_launch(void* const* d_in, const int* in_sizes, int n_in, void* d_out, int out_size, void* d_ws,
                              size_t ws_size, hipStream_t stream) {
    const u16* x = (const u16*)d_in[0];
    const u16* w1 = (const u16*)d_in[1];
    const u16* w2 = (const u16*)d_in[2];
    u16* h = (u16*)d_ws;
    u16* out = (u16*)d_out;
    gemm1_kernel<<<256, 512, 0, stream>>>(x, w1, h, w2);
    gemm2_kernel<<<224, 512, 0, stream>>>(h, w2, out);
}
```

```cpp
#include <hip/hip_runtime.h>
#include <hip/hip_bf16.h>
#include <stdint.h>
#include <type_traits>
#define ST_SC01
#define ROT0

typedef unsigned short u16;
typedef __attribute__((ext_vector_type(8))) short bf16x8;
typedef __attribute__((ext_vector_type(4))) short s16x4;
typedef __attribute__((ext_vector_type(4))) float f32x4;
typedef __attribute__((ext_vector_type(2))) unsigned u32x2;
typedef __attribute__((address_space(3))) const char* lds_cptr;

constexpr int M_ = 2048, D_ = 640, F_ = 1728;

template <int I, int N, class Fn>
__device__ __forceinline__ void static_for(Fn&& f) {
    if constexpr (I < N) { f(std::integral_constant<int, I>{}); static_for<I + 1, N>(f); }
}
template <int N> __device__ __forceinline__ void wait_vm() { asm volatile("s_waitcnt vmcnt(%0)" ::"n"(N) : "memory"); }

__device__ __forceinline__ void glds16(const void* sbase, unsigned voff, unsigned lds_base) {
    unsigned keep;
    asm volatile("s_mov_b32 %0, m0\n\ts_mov_b32 m0, %3\n\ts_nop 0\n\tglobal_load_lds_dwordx4 %1, %2\n\ts_mov_b32 m0, %0"
                 : "=&s"(keep) : "v"(voff), "s"(sbase), "s"(lds_base) : "memory");
}
__device__ __forceinline__ bf16x8 lds_b128(lds_cptr p) { return *(const __attribute__((address_space(3))) bf16x8*)p; }
__device__ __forceinline__ s16x4 lds_tr(lds_cptr p) {
    return __builtin_amdgcn_ds_read_tr16_b64_v4i16((__attribute__((address_space(3))) s16x4*)p);
}
__device__ __forceinline__ void mfma16(f32x4& c, bf16x8 a, bf16x8 b) {
    asm volatile("v_mfma_f32_16x16x32_bf16 %0, %1, %2, %0" : "+a"(c) : "v"(a), "v"(b));
}
__device__ __forceinline__ float bf16_round(float v) { return (float)(__bf16)v; }
__device__ __forceinline__ unsigned pack2(float lo, float hi) {
    typedef __attribute__((ext_vector_type(2))) float f32x2;
    typedef __attribute__((ext_vector_type(2))) __bf16 bf16x2;
    f32x2 v = {lo, hi};
    bf16x2 r = __builtin_convertvector(v, bf16x2);
    return __builtin_bit_cast(unsigned, r);
}
__device__ __forceinline__ float silu_f(float v) { return v * __builtin_amdgcn_rcpf(1.0f + __expf(-v)); }

template <int BM, int BN, int K, int N, int NS, bool SILU>
__device__ __forceinline__ void gemm_tile(const u16* __restrict__ A, const u16* __restrict__ B, u16* __restrict__ C,
                                          char* smem, int m0, int n0, int kshift,
                                          const char* pfa, int pfa_lines, const char* pfb, int slice) {
    constexpr int NK = K / 64;
    constexpr int AB = BM * 128, BB = 64 * BN * 2, ST = AB + BB;
    constexpr int PA = BM / 32, PB = BB / 4096, NI = PA + PB;
    constexpr int WM = BM / 2, WN = BN / 2, TM = WM / 16, TN = WN / 16;
    const int tid = threadIdx.x, lane = tid & 63;
    const int wid = __builtin_amdgcn_readfirstlane(tid >> 6);
    asm volatile("" ::"s"(A), "s"(B), "s"(C));
    if (wid >= 4) {
        const int pw = wid - 4;
        unsigned voff[NI];
#pragma unroll
        for (int i = 0; i < PA; ++i) {
            int row = (pw * PA + i) * 8 + (lane >> 3);
            int ch = (lane & 7) ^ ((row >> 1) & 7);
            if constexpr (SILU) {
                voff[i] = (unsigned)((m0 + row) * (K * 2) + ch * 16);
            } else {
                int rb = (m0 >> 3) + pw * PA + i; rb = rb > M_ / 8 - 1 ? M_ / 8 - 1 : rb;
                voff[i] = (unsigned)(rb * (NK * 1024) + (lane >> 3) * 128 + ch * 16);
            }
        }
#pragma unroll
        for (int i = 0; i < PB; ++i) {
            const int j = pw * PB + i;
            int k, cb;
            if constexpr (BN == 128) { k = j * 4 + (lane >> 4); cb = ((lane & 15) >> 1) ^ ((k & 3) | (((k >> 3) & 1) << 2)); }
            else { k = j * 8 + (lane >> 3); cb = ((lane & 7) >> 1) ^ (((k >> 1) & 1) | (((k >> 3) & 1) << 1)); }
            int col = n0 + cb * 16 + (lane & 1) * 8; col = col > N - 8 ? N - 8 : col;
            voff[PA + i] = (unsigned)(k * (N * 2) + col * 2);
        }
        constexpr int ASTEP = SILU ? 128 : 1024;
        const char* pa = (const char*)A + kshift * ASTEP;
        const char* pb = (const char*)B + (size_t)kshift * (64 * N * 2);
        int kcur = kshift;
        const unsigned da = (unsigned)(uintptr_t)smem + pw * PA * 1024, db = (unsigned)(uintptr_t)smem + AB + pw * PB * 1024;
        unsigned slot = 0;
        auto issue = [&]() {
#pragma unroll
            for (int i = 0; i < PA; ++i) glds16(pa, voff[i], da + slot + i * 1024);
#pragma unroll
            for (int i = 0; i < PB; ++i) glds16(pb, voff[PA + i], db + slot + i * 1024);
            pa += ASTEP; pb += 64 * N * 2;
            if (++kcur == NK) { kcur = 0; pa -= NK * ASTEP; pb -= (size_t)NK * (64 * N * 2); }
            slot += ST; if (slot == NS * ST) slot = 0;
        };
#define PRE 2
#pragma unroll 1
        for (int i = 0; i < PRE; ++i) issue();
        wait_vm<NI*(PRE - 1)>();
        __builtin_amdgcn_s_barrier();
#pragma unroll 1
        for (int i = PRE; i < NS - 1; ++i) issue();
#pragma unroll 1
        for (int kt = 0; kt < NK - NS + 1; ++kt) {
            wait_vm<NI*(NS - 3)>();
            __builtin_amdgcn_s_barrier();
            issue();
        }
        static_for<0, NS - 2>([&](auto J) {
            wait_vm<NI*(NS - 3 - J.value)>();
            __builtin_amdgcn_s_barrier();
        });
        if constexpr (!SILU) return;
    } else {
    unsigned pfd = 0;
    if constexpr (SILU) {
        constexpr int LA = M_ * K * 2 / 128, LB = K * N * 2 / 128;
        int L = (slice * 4 + wid) * 64 + lane; L = L < LA + LB ? L : LA + LB - 1;
        const char* ad = L < LA ? (const char*)A + (size_t)L * 128 : (const char*)B + (size_t)(L - LA) * 128;
        asm volatile("global_load_dword %0, %1, off" : "=v"(pfd) : "v"(ad));
    }
    const int r = lane & 15, g = lane >> 4, q = (lane & 15) >> 2, p = lane & 3;
    const int wm = wid >> 1, wn = wid & 1;
    lds_cptr xa[2];
    {
        const int row = wm * WM + r;
#pragma unroll
        for (int s = 0; s < 2; ++s) xa[s] = (lds_cptr)smem + row * 128 + (((4 * s + g) ^ ((row >> 1) & 7)) * 16);
    }
    lds_cptr wa[2][TN];
#pragma unroll
    for (int t = 0; t < 2; ++t)
#pragma unroll
        for (int nt = 0; nt < TN; ++nt) {
            const int k = 8 * g + 4 * t + q, cb = wn * TN + nt;
            int sl;
            if constexpr (BN == 128) sl = cb ^ ((k & 3) | (((k >> 3) & 1) << 2));
            else sl = cb ^ (((k >> 1) & 1) | (((k >> 3) & 1) << 1));
            wa[t][nt] = (lds_cptr)smem + AB + k * (BN * 2) + sl * 32 + p * 8;
        }
    f32x4 acc[TM][TN];
#pragma unroll
    for (int mt = 0; mt < TM; ++mt)
#pragma unroll
        for (int nt = 0; nt < TN; ++nt) acc[mt][nt] = (f32x4){0.f, 0.f, 0.f, 0.f};

    bf16x8 af[2][TM];
    s16x4 bl[2][TN], bh[2][TN];
    constexpr int NR = TM + 2 * TN, NM = TM * TN;
    auto rd = [&](auto BUF, auto S, auto J, unsigned so) {
        constexpr int buf = BUF.value, s = S.value, j = J.value;
        if constexpr (j >= 2 && j < 2 + TM) af[buf][j - 2] = lds_b128(xa[s] + so + (j - 2) * 2048);
        else {
            constexpr int jb = j < 2 ? j : j - TM;
            if constexpr ((jb & 1) == 0) bl[buf][jb >> 1] = lds_tr(wa[0][jb >> 1] + so + s * 32 * BN * 2);
            else bh[buf][jb >> 1] = lds_tr(wa[1][jb >> 1] + so + s * 32 * BN * 2);
        }
    };
    auto phase = [&](auto BUF, auto SN, unsigned so, auto DORD) {
        constexpr int buf = BUF.value;
        static_for<0, NM>([&](auto J) {
            constexpr int j = J.value, nt = j / TM, mt = j % TM;
            mfma16(acc[mt][nt], __builtin_shufflevector(bl[buf][nt], bh[buf][nt], 0, 1, 2, 3, 4, 5, 6, 7), af[buf][mt]);
            if constexpr (DORD.value) {
                constexpr int NS_ = NM - NM / 4;
                constexpr int lo = (j < NS_ ? j : NS_) * NR / NS_, hi = (j + 1 < NS_ ? j + 1 : NS_) * NR / NS_;
                static_for<lo, hi>([&](auto R) { rd(std::integral_constant<int, buf ^ 1>{}, SN, R, so); });
            }
            __builtin_amdgcn_sched_barrier(0);
        });
    };
    using I0 = std::integral_constant<int, 0>;
    using I1 = std::integral_constant<int, 1>;
    __builtin_amdgcn_s_barrier();
    asm volatile("s_waitcnt vmcnt(0)" ::"v"(pfd) : "memory");
    unsigned so = 0;
    static_for<0, NR>([&](auto R) { rd(I0{}, I0{}, R, 0u); });
#pragma unroll 1
    for (int kt = 0; kt < NK - 1; ++kt) {
        phase(I0{}, I1{}, so, I1{});
        __builtin_amdgcn_s_barrier();
        so += ST; if (so == NS * ST) so = 0;
        phase(I1{}, I0{}, so, I1{});
    }
    phase(I0{}, I1{}, so, I1{});
    phase(I1{}, I0{}, so, I0{});

    asm volatile("s_nop 15\n\ts_nop 15" ::: "memory");
    if constexpr (!SILU) {
#pragma unroll
        for (int mt = 0; mt < TM; ++mt) {
            const int row = m0 + wm * WM + mt * 16 + r;
            u16* crow = C + (size_t)row * N + n0 + wn * WN + 4 * g;
#pragma unroll
            for (int nt = 0; nt < TN; ++nt) {
                if (row < M_ && n0 + wn * WN + nt * 16 < N) {
                    f32x4 v = acc[mt][nt];
                    u32x2 o;
                    o.x = pack2(v[0], v[1]);
                    o.y = pack2(v[2], v[3]);
                    *(u32x2*)(crow + nt * 16) = o;
                }
            }
        }
        return;
    } else {
        static_assert(!SILU || ((NK - 1) % NS != 0 && (NK - 2) % NS != 0 && BM * BN * 2 <= ST && BN == 128), "epilogue slot");
#pragma unroll
        for (int mt = 0; mt < TM; ++mt)
#pragma unroll
            for (int nt = 0; nt < TN; ++nt) {
                f32x4 v = acc[mt][nt];
                u32x2 o;
                o.x = pack2(v[0], v[1]);
                o.y = pack2(v[2], v[3]);
                const int row = wm * WM + mt * 16 + r, c = (wn * WN + nt * 16) / 8 + (g >> 1);
                *(u32x2*)(smem + row * 256 + ((c ^ r) * 16) + (g & 1) * 8) = o;
            }
    }
    }
    if constexpr (SILU) {
        asm volatile("s_waitcnt lgkmcnt(0)" ::: "memory");
        __builtin_amdgcn_s_barrier();
        const int c = lane & 15;
#pragma unroll
        for (int i = 0; i < BM / 32; ++i) {
            const int row = wid * (BM / 8) + i * 4 + (lane >> 4);
            typedef __attribute__((ext_vector_type(4))) unsigned u32x4;
            u32x4 v = *(const u32x4*)(smem + row * 256 + ((c ^ (row & 15)) * 16));
            u32x4 o;
#pragma unroll
            for (int e = 0; e < 4; ++e)
                o[e] = pack2(silu_f(__uint_as_float(v[e] << 16)), silu_f(__uint_as_float(v[e] & 0xffff0000u)));
            if (n0 + c * 8 < N) {
                u32x4* dst = (u32x4*)((char*)C + (size_t)(((m0 + row) >> 3) * (N / 64) + (n0 >> 6) + (c >> 3)) * 1024 + (row & 7) * 128 + (c & 7) * 16);
                asm volatile("global_store_dwordx4 %0, %1, off sc0 sc1\n\ts_nop 1" ::"v"(dst), "v"(o) : "memory");
            }
        }
        if (wid < 4) {   }
    }
}

constexpr int G1_NS = 5;
__global__ __launch_bounds__(512, 2) void gemm1_kernel(const u16* __restrict__ x, const u16* __restrict__ w1, u16* __restrict__ h,
                                                       const u16* __restrict__ w2) {
    __shared__ __attribute__((aligned(128))) char smem[G1_NS * 32768];
    if (blockIdx.x >= 224) {
        const int w = __builtin_amdgcn_readfirstlane(threadIdx.x >> 6);
        const char* p = (const char*)w2 + (size_t)(blockIdx.x - 224) * (F_ * D_ * 2 / 32) + w * 8640;
        const unsigned dst = (unsigned)(uintptr_t)smem + w * 1024;
        const unsigned vo = (threadIdx.x & 63) * 16;
#pragma unroll 1
        for (int i = 0; i < 8; ++i) glds16(p + i * 1024, vo, dst);
        if ((threadIdx.x & 63) < 28) glds16(p + 8192, vo, dst);
        wait_vm<0>();
        return;
    }
    const int b = blockIdx.x, xcd = b & 7, idx = b >> 3;
    const int mb = (xcd >> 1) * 4 + (idx & 3), nb = (xcd & 1) * 7 + (idx >> 2);
    gemm_tile<128, 128, D_, F_, G1_NS, true>(x, w1, h, smem, mb * 128, nb * 128,
                                             0,

                                             (const char*)(x + (size_t)(xcd >> 1) * 512 * D_), 512 * 10, (const char*)(w1 + (xcd & 1) * 896), b);
}

constexpr int G2_NS = 8;
__global__ __launch_bounds__(512, 2) void gemm2_kernel(const u16* __restrict__ h, const u16* __restrict__ w2, u16* __restrict__ out) {
    __shared__ __attribute__((aligned(128))) char smem[G2_NS * 20480];
    const int b = blockIdx.x, xcd = b & 7, idx = b >> 3;
    const int t = xcd * 28 + idx;
    if (t >= 220) return;
    const int mb = t / 10, nb = t - mb * 10;
    const int r0 = (xcd * 28) / 10 * 96, r1x = ((xcd * 28 + 27) / 10 + 1) * 96, r1 = r1x > M_ ? M_ : r1x;
    gemm_tile<96, 64, F_, D_, G2_NS, false>(h, w2, out, smem, mb * 96, nb * 64,
                                            0,

                                            (const char*)(h + (size_t)r0 * F_), (r1 - r0) * 27, (const char*)w2, idx);
}

extern "C" void kernel_launch(void* const* d_in, const int* in_sizes, int n_in, void* d_out, int out_size, void* d_ws,
                              size_t ws_size, hipStream_t stream) {
    const u16* x = (const u16*)d_in[0];
    const u16* w1 = (const u16*)d_in[1];
    const u16* w2 = (const u16*)d_in[2];
    u16* h = (u16*)d_ws;
    u16* out = (u16*)d_out;
    gemm1_kernel<<<256, 512, 0, stream>>>(x, w1, h, w2);
    gemm2_kernel<<<224, 512, 0, stream>>>(h, w2, out);
}
```

```cpp
#include <hip/hip_runtime.h>
#include <hip/hip_bf16.h>
#include <stdint.h>
#include <type_traits>
#define ST_SC01
#define ROT0

typedef unsigned short u16;
typedef __attribute__((ext_vector_type(8))) short bf16x8;
typedef __attribute__((ext_vector_type(4))) short s16x4;
typedef __attribute__((ext_vector_type(4))) float f32x4;
typedef __attribute__((ext_vector_type(2))) unsigned u32x2;
typedef __attribute__((address_space(3))) const char* lds_cptr;

constexpr int M_ = 2048, D_ = 640, F_ = 1728;

template <int I, int N, class Fn>
__device__ __forceinline__ void static_for(Fn&& f) {
    if constexpr (I < N) { f(std::integral_constant<int, I>{}); static_for<I + 1, N>(f); }
}
template <int N> __device__ __forceinline__ void wait_vm() { asm volatile("s_waitcnt vmcnt(%0)" ::"n"(N) : "memory"); }

__device__ __forceinline__ void glds16(const void* sbase, unsigned voff, unsigned lds_base) {
    unsigned keep;
    asm volatile("s_mov_b32 %0, m0\n\ts_mov_b32 m0, %3\n\ts_nop 0\n\tglobal_load_lds_dwordx4 %1, %2\n\ts_mov_b32 m0, %0"
                 : "=&s"(keep) : "v"(voff), "s"(sbase), "s"(lds_base) : "memory");
}
__device__ __forceinline__ bf16x8 lds_b128(lds_cptr p) { return *(const __attribute__((address_space(3))) bf16x8*)p; }
__device__ __forceinline__ s16x4 lds_tr(lds_cptr p) {
    return __builtin_amdgcn_ds_read_tr16_b64_v4i16((__attribute__((address_space(3))) s16x4*)p);
}
__device__ __forceinline__ void mfma16(f32x4& c, bf16x8 a, bf16x8 b) {
    asm volatile("v_mfma_f32_16x16x32_bf16 %0, %1, %2, %0" : "+a"(c) : "v"(a), "v"(b));
}
__device__ __forceinline__ float bf16_round(float v) { return (float)(__bf16)v; }
__device__ __forceinline__ unsigned pack2(float lo, float hi) {
    typedef __attribute__((ext_vector_type(2))) float f32x2;
    typedef __attribute__((ext_vector_type(2))) __bf16 bf16x2;
    f32x2 v = {lo, hi};
    bf16x2 r = __builtin_convertvector(v, bf16x2);
    return __builtin_bit_cast(unsigned, r);
}
__device__ __forceinline__ float silu_f(float v) { return v * __builtin_amdgcn_rcpf(1.0f + __expf(-v)); }

template <int BM, int BN, int K, int N, int NS, bool SILU>
__device__ __forceinline__ void gemm_tile(const u16* __restrict__ A, const u16* __restrict__ B, u16* __restrict__ C,
                                          char* smem, int m0, int n0, int kshift,
                                          const char* tlbp, int slice) {
    constexpr int NK = K / 64;
    constexpr int AB = BM * 128, BB = 64 * BN * 2, ST = AB + BB;
    constexpr int PA = BM / 32, PB = BB / 4096, NI = PA + PB;
    constexpr int WM = BM / 2, WN = BN / 2, TM = WM / 16, TN = WN / 16;
    const int tid = threadIdx.x, lane = tid & 63;
    const int wid = __builtin_amdgcn_readfirstlane(tid >> 6);
    asm volatile("" ::"s"(A), "s"(B), "s"(C));
    if (wid >= 4) {
        const int pw = wid - 4;
        unsigned voff[NI];
#pragma unroll
        for (int i = 0; i < PA; ++i) {
            int row = (pw * PA + i) * 8 + (lane >> 3);
            int ch = (lane & 7) ^ ((row >> 1) & 7);
            if constexpr (SILU) {
                voff[i] = (unsigned)((m0 + row) * (K * 2) + ch * 16);
            } else {
                int rb = (m0 >> 3) + pw * PA + i; rb = rb > M_ / 8 - 1 ? M_ / 8 - 1 : rb;
                voff[i] = (unsigned)(rb * (NK * 1024) + (lane >> 3) * 128 + ch * 16);
            }
        }
#pragma unroll
        for (int i = 0; i < PB; ++i) {
            const int j = pw * PB + i;
            int k, cb;
            if constexpr (BN == 128) { k = j * 4 + (lane >> 4); cb = ((lane & 15) >> 1) ^ ((k & 3) | (((k >> 3) & 1) << 2)); }
            else { k = j * 8 + (lane >> 3); cb = ((lane & 7) >> 1) ^ (((k >> 1) & 1) | (((k >> 3) & 1) << 1)); }
            int col = n0 + cb * 16 + (lane & 1) * 8; col = col > N - 8 ? N - 8 : col;
            voff[PA + i] = (unsigned)(k * (N * 2) + col * 2);
        }
        constexpr int ASTEP = SILU ? 128 : 1024;
        const char* pa = (const char*)A + kshift * ASTEP;
        const char* pb = (const char*)B + (size_t)kshift * (64 * N * 2);
        int kcur = kshift;
        const unsigned da = (unsigned)(uintptr_t)smem + pw * PA * 1024, db = (unsigned)(uintptr_t)smem + AB + pw * PB * 1024;
        unsigned slot = 0;
        auto issue = [&]() {
#pragma unroll
            for (int i = 0; i < PA; ++i) glds16(pa, voff[i], da + slot + i * 1024);
#pragma unroll
            for (int i = 0; i < PB; ++i) glds16(pb, voff[PA + i], db + slot + i * 1024);
            pa += ASTEP; pb += 64 * N * 2;
            if (++kcur == NK) { kcur = 0; pa -= NK * ASTEP; pb -= (size_t)NK * (64 * N * 2); }
            slot += ST; if (slot == NS * ST) slot = 0;
        };
#define PRE 2
#pragma unroll 1
        for (int i = 0; i < PRE; ++i) issue();
        wait_vm<NI*(PRE - 1)>();
        __builtin_amdgcn_s_barrier();
#pragma unroll 1
        for (int i = PRE; i < NS - 1; ++i) issue();
#pragma unroll 1
        for (int kt = 0; kt < NK - NS + 1; ++kt) {
            wait_vm<NI*(NS - 3)>();
            __builtin_amdgcn_s_barrier();
            issue();
        }
        static_for<0, NS - 2>([&](auto J) {
            wait_vm<NI*(NS - 3 - J.value)>();
            __builtin_amdgcn_s_barrier();
        });
        if constexpr (!SILU) return;
    } else {
    unsigned pfd = 0, pfd2 = 0;
    if constexpr (SILU) {
        constexpr int LA = M_ * K * 2 / 128, LB = K * N * 2 / 128;
        int L = (slice * 4 + wid) * 64 + lane; L = L < LA + LB ? L : LA + LB - 1;
        const char* ad = L < LA ? (const char*)A + (size_t)L * 128 : (const char*)B + (size_t)(L - LA) * 128;
        asm volatile("global_load_dword %0, %1, off" : "=v"(pfd) : "v"(ad));
        if (wid == 1) asm volatile("global_load_dword %0, %1, off" : "=v"(pfd2) : "v"(tlbp));
    }
    const int r = lane & 15, g = lane >> 4, q = (lane & 15) >> 2, p = lane & 3;
    const int wm = wid >> 1, wn = wid & 1;
    lds_cptr xa[2];
    {
        const int row = wm * WM + r;
#pragma unroll
        for (int s = 0; s < 2; ++s) xa[s] = (lds_cptr)smem + row * 128 + (((4 * s + g) ^ ((row >> 1) & 7)) * 16);
    }
    lds_cptr wa[2][TN];
#pragma unroll
    for (int t = 0; t < 2; ++t)
#pragma unroll
        for (int nt = 0; nt < TN; ++nt) {
            const int k = 8 * g + 4 * t + q, cb = wn * TN + nt;
            int sl;
            if constexpr (BN == 128) sl = cb ^ ((k & 3) | (((k >> 3) & 1) << 2));
            else sl = cb ^ (((k >> 1) & 1) | (((k >> 3) & 1) << 1));
            wa[t][nt] = (lds_cptr)smem + AB + k * (BN * 2) + sl * 32 + p * 8;
        }
    f32x4 acc[TM][TN];
#pragma unroll
    for (int mt = 0; mt < TM; ++mt)
#pragma unroll
        for (int nt = 0; nt < TN; ++nt) acc[mt][nt] = (f32x4){0.f, 0.f, 0.f, 0.f};

    bf16x8 af[2][TM];
    s16x4 bl[2][TN], bh[2][TN];
    constexpr int NR = TM + 2 * TN, NM = TM * TN;
    auto rd = [&](auto BUF, auto S, auto J, unsigned so) {
        constexpr int buf = BUF.value, s = S.value, j = J.value;
        if constexpr (j >= 2 && j < 2 + TM) af[buf][j - 2] = lds_b128(xa[s] + so + (j - 2) * 2048);
        else {
            constexpr int jb = j < 2 ? j : j - TM;
            if constexpr ((jb & 1) == 0) bl[buf][jb >> 1] = lds_tr(wa[0][jb >> 1] + so + s * 32 * BN * 2);
            else bh[buf][jb >> 1] = lds_tr(wa[1][jb >> 1] + so + s * 32 * BN * 2);
        }
    };
    auto phase = [&](auto BUF, auto SN, unsigned so, auto DORD) {
        constexpr int buf = BUF.value;
        static_for<0, NM>([&](auto J) {
            constexpr int j = J.value, nt = j / TM, mt = j % TM;
            mfma16(acc[mt][nt], __builtin_shufflevector(bl[buf][nt], bh[buf][nt], 0, 1, 2, 3, 4, 5, 6, 7), af[buf][mt]);
            if constexpr (DORD.value) {
                constexpr int NS_ = NM - NM / 4;
                constexpr int lo = (j < NS_ ? j : NS_) * NR / NS_, hi = (j + 1 < NS_ ? j + 1 : NS_) * NR / NS_;
                static_for<lo, hi>([&](auto R) { rd(std::integral_constant<int, buf ^ 1>{}, SN, R, so); });
            }
            __builtin_amdgcn_sched_barrier(0);
        });
    };
    using I0 = std::integral_constant<int, 0>;
    using I1 = std::integral_constant<int, 1>;
    __builtin_amdgcn_s_barrier();
    asm volatile("s_waitcnt vmcnt(0)" ::"v"(pfd), "v"(pfd2) : "memory");
    unsigned so = 0;
    static_for<0, NR>([&](auto R) { rd(I0{}, I0{}, R, 0u); });
#pragma unroll 1
    for (int kt = 0; kt < NK - 1; ++kt) {
        phase(I0{}, I1{}, so, I1{});
        __builtin_amdgcn_s_barrier();
        so += ST; if (so == NS * ST) so = 0;
        phase(I1{}, I0{}, so, I1{});
    }
    phase(I0{}, I1{}, so, I1{});
    phase(I1{}, I0{}, so, I0{});

    asm volatile("s_nop 15\n\ts_nop 15" ::: "memory");
    if constexpr (!SILU) {
#pragma unroll
        for (int mt = 0; mt < TM; ++mt) {
            const int row = m0 + wm * WM + mt * 16 + r;
            u16* crow = C + (size_t)row * N + n0 + wn * WN + 4 * g;
#pragma unroll
            for (int nt = 0; nt < TN; ++nt) {
                if (row < M_ && n0 + wn * WN + nt * 16 < N) {
                    f32x4 v = acc[mt][nt];
                    u32x2 o;
                    o.x = pack2(v[0], v[1]);
                    o.y = pack2(v[2], v[3]);
                    *(u32x2*)(crow + nt * 16) = o;
                }
            }
        }
        return;
    } else {
        static_assert(!SILU || ((NK - 1) % NS != 0 && (NK - 2) % NS != 0 && BM * BN * 2 <= ST && BN == 128), "epilogue slot");
#pragma unroll
        for (int mt = 0; mt < TM; ++mt)
#pragma unroll
            for (int nt = 0; nt < TN; ++nt) {
                f32x4 v = acc[mt][nt];
                u32x2 o;
                o.x = pack2(v[0], v[1]);
                o.y = pack2(v[2], v[3]);
                const int row = wm * WM + mt * 16 + r, c = (wn * WN + nt * 16) / 8 + (g >> 1);
                *(u32x2*)(smem + row * 256 + ((c ^ r) * 16) + (g & 1) * 8) = o;
            }
    }
    }
    if constexpr (SILU) {
        asm volatile("s_waitcnt lgkmcnt(0)" ::: "memory");
        __builtin_amdgcn_s_barrier();
        const int c = lane & 15;
#pragma unroll
        for (int i = 0; i < BM / 32; ++i) {
            const int row = wid * (BM / 8) + i * 4 + (lane >> 4);
            typedef __attribute__((ext_vector_type(4))) unsigned u32x4;
            u32x4 v = *(const u32x4*)(smem + row * 256 + ((c ^ (row & 15)) * 16));
            u32x4 o;
#pragma unroll
            for (int e = 0; e < 4; ++e)
                o[e] = pack2(silu_f(__uint_as_float(v[e] << 16)), silu_f(__uint_as_float(v[e] & 0xffff0000u)));
            if (n0 + c * 8 < N) {
                u32x4* dst = (u32x4*)((char*)C + (size_t)(((m0 + row) >> 3) * (N / 64) + (n0 >> 6) + (c >> 3)) * 1024 + (row & 7) * 128 + (c & 7) * 16);
                asm volatile("global_store_dwordx4 %0, %1, off sc0 sc1\n\ts_nop 1" ::"v"(dst), "v"(o) : "memory");
            }
        }
        if (wid < 4) {   }
    }
}

constexpr int G1_NS = 5;
__global__ __launch_bounds__(512, 2) void gemm1_kernel(const u16* __restrict__ x, const u16* __restrict__ w1, u16* __restrict__ h,
                                                       const u16* __restrict__ w2, const u16* __restrict__ out) {
    __shared__ __attribute__((aligned(128))) char smem[G1_NS * 32768];
    if (blockIdx.x >= 224) {
        const int w = __builtin_amdgcn_readfirstlane(threadIdx.x >> 6);
        const char* p = (const char*)w2 + (size_t)(blockIdx.x - 224) * (F_ * D_ * 2 / 32) + w * 8640;
        const unsigned dst = (unsigned)(uintptr_t)smem + w * 1024;
        const unsigned vo = (threadIdx.x & 63) * 16;
#pragma unroll 1
        for (int i = 0; i < 8; ++i) glds16(p + i * 1024, vo, dst);
        if ((threadIdx.x & 63) < 28) glds16(p + 8192, vo, dst);
        wait_vm<0>();
        return;
    }
    const int b = blockIdx.x, xcd = b & 7, idx = b >> 3;
    const int mb = (xcd >> 1) * 4 + (idx & 3), nb = (xcd & 1) * 7 + (idx >> 2);
    const char* tlbp;
    {
        const int L = idx * 64 + (threadIdx.x & 63);
        const int r0 = (xcd * 28) / 10 * 96;
        if (L < 540) tlbp = (const char*)w2 + (size_t)L * 4096;
        else if (L < 1180) tlbp = (const char*)out + (size_t)(L - 540) * 4096;
        else { size_t o = (size_t)r0 * (F_ * 2) + (size_t)(L - 1180) * 4096; o = o < (size_t)M_ * F_ * 2 ? o : 0; tlbp = (const char*)h + o; }
    }
    gemm_tile<128, 128, D_, F_, G1_NS, true>(x, w1, h, smem, mb * 128, nb * 128,
                                             0,

                                             tlbp, b);
}

constexpr int G2_NS = 8;
__global__ __launch_bounds__(512, 2) void gemm2_kernel(const u16* __restrict__ h, const u16* __restrict__ w2, u16* __restrict__ out) {
    __shared__ __attribute__((aligned(128))) char smem[G2_NS * 20480];
    const int b = blockIdx.x, xcd = b & 7, idx = b >> 3;
    const int t = xcd * 28 + idx;
    if (t >= 220) return;
    const int mb = t / 10, nb = t - mb * 10;
    gemm_tile<96, 64, F_, D_, G2_NS, false>(h, w2, out, smem, mb * 96, nb * 64,
                                            0,

                                            nullptr, idx);
}

extern "C" void kernel_launch(void* const* d_in, const int* in_sizes, int n_in, void* d_out, int out_size, void* d_ws,
                              size_t ws_size, hipStream_t stream) {
    const u16* x = (const u16*)d_in[0];
    const u16* w1 = (const u16*)d_in[1];
    const u16* w2 = (const u16*)d_in[2];
    u16* h = (u16*)d_ws;
    u16* out = (u16*)d_out;
    gemm1_kernel<<<256, 512, 0, stream>>>(x, w1, h, w2, out);
    gemm2_kernel<<<224, 512, 0, stream>>>(h, w2, out);
}
```

```cpp
#include <hip/hip_runtime.h>
#include <hip/hip_bf16.h>
#include <stdint.h>
#include <type_traits>

typedef unsigned short u16;
typedef __attribute__((ext_vector_type(8))) short bf16x8;
typedef __attribute__((ext_vector_type(4))) short s16x4;
typedef __attribute__((ext_vector_type(4))) float f32x4;
typedef __attribute__((ext_vector_type(2))) unsigned u32x2;
typedef __attribute__((ext_vector_type(4))) unsigned u32x4;
typedef __attribute__((address_space(3))) const char* lds_cptr;

constexpr int M_ = 2048, D_ = 640, F_ = 1728;

template <int I, int N, class Fn>
__device__ __forceinline__ void static_for(Fn&& f) {
    if constexpr (I < N) { f(std::integral_constant<int, I>{}); static_for<I + 1, N>(f); }
}
template <int N> __device__ __forceinline__ void wait_vm() { asm volatile("s_waitcnt vmcnt(%0)" ::"n"(N) : "memory"); }

__device__ __forceinline__ void glds16(const void* sbase, unsigned voff, unsigned lds_base) {
    unsigned keep;
    asm volatile("s_mov_b32 %0, m0\n\ts_mov_b32 m0, %3\n\ts_nop 0\n\tglobal_load_lds_dwordx4 %1, %2\n\ts_mov_b32 m0, %0"
                 : "=&s"(keep) : "v"(voff), "s"(sbase), "s"(lds_base) : "memory");
}
__device__ __forceinline__ bf16x8 lds_b128(lds_cptr p) { return *(const __attribute__((address_space(3))) bf16x8*)p; }
__device__ __forceinline__ s16x4 lds_tr(lds_cptr p) {
    return __builtin_amdgcn_ds_read_tr16_b64_v4i16((__attribute__((address_space(3))) s16x4*)p);
}
__device__ __forceinline__ void mfma16(f32x4& c, bf16x8 a, bf16x8 b) {
    asm volatile("v_mfma_f32_16x16x32_bf16 %0, %1, %2, %0" : "+a"(c) : "v"(a), "v"(b));
}
__device__ __forceinline__ unsigned pack2(float lo, float hi) {
    typedef __attribute__((ext_vector_type(2))) float f32x2;
    typedef __attribute__((ext_vector_type(2))) __bf16 bf16x2;
    f32x2 v = {lo, hi};
    bf16x2 r = __builtin_convertvector(v, bf16x2);
    return __builtin_bit_cast(unsigned, r);
}
__device__ __forceinline__ float silu_f(float v) { return v * __builtin_amdgcn_rcpf(1.0f + __expf(-v)); }

template <int BM, int BN, int K, int N, int NS, bool SILU>
__device__ __forceinline__ void gemm_tile(const u16* __restrict__ A, const u16* __restrict__ B, u16* __restrict__ C,
                                          char* smem, int m0, int n0, int slice) {
    constexpr int NK = K / 64;
    constexpr int AB = BM * 128, BB = 64 * BN * 2, ST = AB + BB;
    constexpr int PA = BM / 32, PB = BB / 4096, NI = PA + PB;
    constexpr int WM = BM / 2, WN = BN / 2, TM = WM / 16, TN = WN / 16;
    const int tid = threadIdx.x, lane = tid & 63;
    const int wid = __builtin_amdgcn_readfirstlane(tid >> 6);
    if (wid >= 4) {
        const int pw = wid - 4;
        unsigned voff[NI];
#pragma unroll
        for (int i = 0; i < PA; ++i) {
            const int row = (pw * PA + i) * 8 + (lane >> 3);
            const int ch = (lane & 7) ^ ((row >> 1) & 7);
            if constexpr (SILU) {
                voff[i] = (unsigned)((m0 + row) * (K * 2) + ch * 16);
            } else {
                int rb = (m0 >> 3) + pw * PA + i; rb = rb > M_ / 8 - 1 ? M_ / 8 - 1 : rb;
                voff[i] = (unsigned)(rb * (NK * 1024) + (lane >> 3) * 128 + ch * 16);
            }
        }
#pragma unroll
        for (int i = 0; i < PB; ++i) {
            const int j = pw * PB + i;
            int k, cb;
            if constexpr (BN == 128) { k = j * 4 + (lane >> 4); cb = ((lane & 15) >> 1) ^ ((k & 3) | (((k >> 3) & 1) << 2)); }
            else { k = j * 8 + (lane >> 3); cb = ((lane & 7) >> 1) ^ (((k >> 1) & 1) | (((k >> 3) & 1) << 1)); }
            int col = n0 + cb * 16 + (lane & 1) * 8; col = col > N - 8 ? N - 8 : col;
            voff[PA + i] = (unsigned)(k * (N * 2) + col * 2);
        }
        constexpr int ASTEP = SILU ? 128 : 1024;
        const char* pa = (const char*)A;
        const char* pb = (const char*)B;
        const unsigned da = (unsigned)(uintptr_t)smem + pw * PA * 1024, db = (unsigned)(uintptr_t)smem + AB + pw * PB * 1024;
        unsigned slot = 0;
        auto issue = [&]() {
#pragma unroll
            for (int i = 0; i < PA; ++i) glds16(pa, voff[i], da + slot + i * 1024);
#pragma unroll
            for (int i = 0; i < PB; ++i) glds16(pb, voff[PA + i], db + slot + i * 1024);
            pa += ASTEP; pb += 64 * N * 2;
            slot += ST; if (slot == NS * ST) slot = 0;
        };
        issue(); issue();
        wait_vm<NI>();
        __builtin_amdgcn_s_barrier();
#pragma unroll 1
        for (int i = 2; i < NS - 1; ++i) issue();
#pragma unroll 1
        for (int kt = 0; kt < NK - NS + 1; ++kt) {
            wait_vm<NI*(NS - 3)>();
            __builtin_amdgcn_s_barrier();
            issue();
        }
        static_for<0, NS - 2>([&](auto J) {
            wait_vm<NI*(NS - 3 - J.value)>();
            __builtin_amdgcn_s_barrier();
        });
        if constexpr (!SILU) return;
    } else {
        unsigned pfd = 0;
        if constexpr (SILU) {
            constexpr int LA = M_ * K * 2 / 128, LB = K * N * 2 / 128;
            int L = (slice * 4 + wid) * 64 + lane; L = L < LA + LB ? L : LA + LB - 1;
            const char* ad = L < LA ? (const char*)A + (size_t)L * 128 : (const char*)B + (size_t)(L - LA) * 128;
            asm volatile("global_load_dword %0, %1, off" : "=v"(pfd) : "v"(ad));
        }
        const int r = lane & 15, g = lane >> 4, q = (lane & 15) >> 2, p = lane & 3;
        const int wm = wid >> 1, wn = wid & 1;
        lds_cptr xa[2];
        {
            const int row = wm * WM + r;
#pragma unroll
            for (int s = 0; s < 2; ++s) xa[s] = (lds_cptr)smem + row * 128 + (((4 * s + g) ^ ((row >> 1) & 7)) * 16);
        }
        lds_cptr wa[2][TN];
#pragma unroll
        for (int t = 0; t < 2; ++t)
#pragma unroll
            for (int nt = 0; nt < TN; ++nt) {
                const int k = 8 * g + 4 * t + q, cb = wn * TN + nt;
                int sl;
                if constexpr (BN == 128) sl = cb ^ ((k & 3) | (((k >> 3) & 1) << 2));
                else sl = cb ^ (((k >> 1) & 1) | (((k >> 3) & 1) << 1));
                wa[t][nt] = (lds_cptr)smem + AB + k * (BN * 2) + sl * 32 + p * 8;
            }
        f32x4 acc[TM][TN];
#pragma unroll
        for (int mt = 0; mt < TM; ++mt)
#pragma unroll
            for (int nt = 0; nt < TN; ++nt) acc[mt][nt] = (f32x4){0.f, 0.f, 0.f, 0.f};

        bf16x8 af[2][TM];
        s16x4 bl[2][TN], bh[2][TN];
        constexpr int NR = TM + 2 * TN, NM = TM * TN;
        auto rd = [&](auto BUF, auto S, auto J, unsigned so) {
            constexpr int buf = BUF.value, s = S.value, j = J.value;
            if constexpr (j >= 2 && j < 2 + TM) af[buf][j - 2] = lds_b128(xa[s] + so + (j - 2) * 2048);
            else {
                constexpr int jb = j < 2 ? j : j - TM;
                if constexpr ((jb & 1) == 0) bl[buf][jb >> 1] = lds_tr(wa[0][jb >> 1] + so + s * 32 * BN * 2);
                else bh[buf][jb >> 1] = lds_tr(wa[1][jb >> 1] + so + s * 32 * BN * 2);
            }
        };
        auto phase = [&](auto BUF, auto SN, unsigned so, auto DORD) {
            constexpr int buf = BUF.value;
            static_for<0, NM>([&](auto J) {
                constexpr int j = J.value, nt = j / TM, mt = j % TM;
                mfma16(acc[mt][nt], __builtin_shufflevector(bl[buf][nt], bh[buf][nt], 0, 1, 2, 3, 4, 5, 6, 7), af[buf][mt]);
                if constexpr (DORD.value) {
                    constexpr int NS_ = NM - NM / 4;
                    constexpr int lo = (j < NS_ ? j : NS_) * NR / NS_, hi = (j + 1 < NS_ ? j + 1 : NS_) * NR / NS_;
                    static_for<lo, hi>([&](auto R) { rd(std::integral_constant<int, buf ^ 1>{}, SN, R, so); });
                }
                __builtin_amdgcn_sched_barrier(0);
            });
        };
        using I0 = std::integral_constant<int, 0>;
        using I1 = std::integral_constant<int, 1>;
        __builtin_amdgcn_s_barrier();
        asm volatile("s_waitcnt vmcnt(0)" ::"v"(pfd) : "memory");
        unsigned so = 0;
        static_for<0, NR>([&](auto R) { rd(I0{}, I0{}, R, 0u); });
#pragma unroll 1
        for (int kt = 0; kt < NK - 1; ++kt) {
            phase(I0{}, I1{}, so, I1{});
            __builtin_amdgcn_s_barrier();
            so += ST; if (so == NS * ST) so = 0;
            phase(I1{}, I0{}, so, I1{});
        }
        phase(I0{}, I1{}, so, I1{});
        phase(I1{}, I0{}, so, I0{});

        asm volatile("s_nop 15\n\ts_nop 15" ::: "memory");
        if constexpr (!SILU) {
#pragma unroll
            for (int mt = 0; mt < TM; ++mt) {
                const int row = m0 + wm * WM + mt * 16 + r;
                u16* crow = C + (size_t)row * N + n0 + wn * WN + 4 * g;
#pragma unroll
                for (int nt = 0; nt < TN; ++nt) {
                    if (row < M_ && n0 + wn * WN + nt * 16 < N) {
                        f32x4 v = acc[mt][nt];
                        u32x2 o;
                        o.x = pack2(v[0], v[1]);
                        o.y = pack2(v[2], v[3]);
                        *(u32x2*)(crow + nt * 16) = o;
                    }
                }
            }
            return;
        } else {
            static_assert(!SILU || ((NK - 1) % NS != 0 && (NK - 2) % NS != 0 && BM * BN * 2 <= ST && BN == 128), "epilogue slot");
#pragma unroll
            for (int mt = 0; mt < TM; ++mt)
#pragma unroll
                for (int nt = 0; nt < TN; ++nt) {
                    f32x4 v = acc[mt][nt];
                    u32x2 o;
                    o.x = pack2(v[0], v[1]);
                    o.y = pack2(v[2], v[3]);
                    const int row = wm * WM + mt * 16 + r, c = (wn * WN + nt * 16) / 8 + (g >> 1);
                    *(u32x2*)(smem + row * 256 + ((c ^ r) * 16) + (g & 1) * 8) = o;
                }
        }
    }
    if constexpr (SILU) {
        asm volatile("s_waitcnt lgkmcnt(0)" ::: "memory");
        __builtin_amdgcn_s_barrier();
        const int c = lane & 15;
#pragma unroll
        for (int i = 0; i < BM / 32; ++i) {
            const int row = wid * (BM / 8) + i * 4 + (lane >> 4);
            u32x4 v = *(const u32x4*)(smem + row * 256 + ((c ^ (row & 15)) * 16));
            u32x4 o;
#pragma unroll
            for (int e = 0; e < 4; ++e)
                o[e] = pack2(silu_f(__uint_as_float(v[e] << 16)), silu_f(__uint_as_float(v[e] & 0xffff0000u)));
            if (n0 + c * 8 < N) {
                u32x4* dst = (u32x4*)((char*)C + (size_t)(((m0 + row) >> 3) * (N / 64) + (n0 >> 6) + (c >> 3)) * 1024 + (row & 7) * 128 + (c & 7) * 16);
                asm volatile("global_store_dwordx4 %0, %1, off sc0 sc1\n\ts_nop 1" ::"v"(dst), "v"(o) : "memory");
            }
        }
        if (wid < 4) {   }
    }
}

constexpr int G1_NS = 5;
__global__ __launch_bounds__(512, 2) void gemm1_kernel(const u16* __restrict__ x, const u16* __restrict__ w1, u16* __restrict__ h,
                                                       const u16* __restrict__ w2) {
    __shared__ __attribute__((aligned(128))) char smem[G1_NS * 32768];
    if (blockIdx.x >= 224) {
        const int w = __builtin_amdgcn_readfirstlane(threadIdx.x >> 6);
        const char* p = (const char*)w2 + (size_t)(blockIdx.x - 224) * (F_ * D_ * 2 / 32) + w * 8640;
        const unsigned dst = (unsigned)(uintptr_t)smem + w * 1024;
        const unsigned vo = (threadIdx.x & 63) * 16;
#pragma unroll 1
        for (int i = 0; i < 8; ++i) glds16(p + i * 1024, vo, dst);
        if ((threadIdx.x & 63) < 28) glds16(p + 8192, vo, dst);
        wait_vm<0>();
        return;
    }
    const int b = blockIdx.x, xcd = b & 7, idx = b >> 3;
    const int mb = (xcd >> 1) * 4 + (idx & 3), nb = (xcd & 1) * 7 + (idx >> 2);
    gemm_tile<128, 128, D_, F_, G1_NS, true>(x, w1, h, smem, mb * 128, nb * 128, b);
}

constexpr int G2_NS = 8;
__global__ __launch_bounds__(512, 2) void gemm2_kernel(const u16* __restrict__ h, const u16* __restrict__ w2, u16* __restrict__ out) {
    __shared__ __attribute__((aligned(128))) char smem[G2_NS * 20480];
    const int b = blockIdx.x, xcd = b & 7, idx = b >> 3;
    const int t = xcd * 28 + idx;
    if (t >= 220) return;
    const int mb = t / 10, nb = t - mb * 10;
    gemm_tile<96, 64, F_, D_, G2_NS, false>(h, w2, out, smem, mb * 96, nb * 64, idx);
}

extern "C" void kernel_launch(void* const* d_in, const int* in_sizes, int n_in, void* d_out, int out_size, void* d_ws,
                              size_t ws_size, hipStream_t stream) {
    const u16* x = (const u16*)d_in[0];
    const u16* w1 = (const u16*)d_in[1];
    const u16* w2 = (const u16*)d_in[2];
    u16* h = (u16*)d_ws;
    u16* out = (u16*)d_out;
    gemm1_kernel<<<256, 512, 0, stream>>>(x, w1, h, w2);
    gemm2_kernel<<<224, 512, 0, stream>>>(h, w2, out);
}
```
